```python
import torch

def _softmax(x, axis=-1):
    return torch.softmax(x, dim=axis)
def _gelu(x):
    return torch.nn.functional.gelu(x, approximate="tanh")
def _silu(x):
    return torch.nn.functional.silu(x)
def _sigmoid(x):
    return torch.sigmoid(x)
def _layernorm(x, axis=-1, eps=1e-5):
    m = x.mean(dim=axis, keepdim=True)
    v = x.var(dim=axis, keepdim=True, unbiased=False)
    return (x - m) / torch.sqrt(v + eps)
def _scatter_add(tgt, idx, src, axis=0):
    return tgt.clone().index_add_(axis, idx, src)
def _segment_sum(src, seg, n):
    out = torch.zeros((n,) + src.shape[1:], dtype=src.dtype)
    return out.index_add_(0, seg, src)
def _pad(x, pw):
    flat = tuple(v for pair in reversed(pw) for v in pair)
    return torch.nn.functional.pad(x, flat)

kernel_name = 'gated_mix__sliding_block_cached__glu_gated_swiglu__decode'

def setup_inputs():

    _w = (lambda a: (lambda t: t.to(torch.bfloat16) if t.is_floating_point() else t)(torch.as_tensor(a)))
    def _A_setup():
        B, Sq, D = 8, 1, 640
        Hq, Hkv, Sk, Dh = 32, 32, 4096, 128
        assert Hq % Hkv == 0, (Hq, Hkv)
        return {
            "x": _w((lambda *s: torch.randn(*s, dtype=torch.bfloat16) * 0.05)(B, Sq, D)),
            "k_cache": _w((lambda *s: torch.randn(*s, dtype=torch.bfloat16) * 6.4)(B, Hkv, Sk, Dh)),
            "v_cache": _w((lambda *s: torch.randn(*s, dtype=torch.bfloat16) * 0.05)(B, Hkv, Sk, Dh)),
            "wq": _w((lambda *s: torch.randn(*s, dtype=torch.bfloat16) * 6.4)(D, Hq * Dh) * (D ** -0.5)),
            "wo": _w((lambda *s: torch.randn(*s, dtype=torch.bfloat16) * 0.05)(Hq * Dh, D) * ((Hq * Dh) ** -0.5)),
        }
    _A_in = _A_setup()

    def _B_setup():
        B, S, D, F = 8, 1, 640, 1728
        return {
            "x": _w((lambda *s: torch.randn(*s, dtype=torch.bfloat16) * 0.05)(B, S, D)),
            "w1": _w((lambda *s: torch.randn(*s, dtype=torch.bfloat16) * 0.4)(D, F) * (2.0 / D) ** 0.5),
            "w3": _w((lambda *s: torch.randn(*s, dtype=torch.bfloat16) * 0.4)(D, F) * (2.0 / D) ** 0.5),
            "w2": _w((lambda *s: torch.randn(*s, dtype=torch.bfloat16) * 0.4)(F, D) * (2.0 / F) ** 0.5),
        }
    _B_in = _B_setup()

    return {
        "A_x": _A_in["x"],
        "A_k_cache": _A_in["k_cache"],
        "A_v_cache": _A_in["v_cache"],
        "A_wq": _A_in["wq"],
        "A_wo": _A_in["wo"],
        "B_x": _B_in["x"],
        "B_w1": _B_in["w1"],
        "B_w3": _B_in["w3"],
        "B_w2": _B_in["w2"],
        "gate_w": _w((lambda *s: torch.randn(*s, dtype=torch.bfloat16) * 0.05)(640) * 16.0),
    }

def reference(A_x, A_k_cache, A_v_cache, A_wq, A_wo, B_x, B_w1, B_w3, B_w2, gate_w):
    def _A_ref(x, k_cache, v_cache, wq, wo):
        B, Sq, D = x.shape
        Hq, Dh = 32, 128
        Hkv, Sk = k_cache.shape[1], k_cache.shape[2]
        W = min(4096, max(256, Sk // 4))
        rep = Hq // Hkv
        q = (lambda x, *d: x.permute(*d))((x @ wq).reshape(B, Sq, Hq, Dh), 0, 2, 1, 3)
        k = (lambda x, n, axis: torch.repeat_interleave(x, n, dim=axis))(k_cache[:, :, Sk - W:, :], rep, axis=1)
        v = (lambda x, n, axis: torch.repeat_interleave(x, n, dim=axis))(v_cache[:, :, Sk - W:, :], rep, axis=1)
        scores = torch.einsum("bhqd,bhkd->bhqk", q, k) * (Dh ** -0.5)
        p = _softmax(scores, axis=-1)
        o = torch.einsum("bhqk,bhkd->bhqd", p, v)
        return (lambda x, *d: x.permute(*d))(o, 0, 2, 1, 3).reshape(B, Sq, Hq * Dh) @ wo

    def _B_ref(x, w1, w3, w2):
        gate = torch.einsum("bsd,df->bsf", x, w1)
        up = torch.einsum("bsd,df->bsf", x, w3)
        h = _silu(gate) * up
        out = torch.einsum("bsf,fd->bsd", h, w2)
        return out

    x = A_x
    g = _sigmoid((x * gate_w).sum(-1, keepdims=True))
    a = _A_ref(x, A_k_cache, A_v_cache, A_wq, A_wo)
    b = _B_ref(x, B_w1, B_w3, B_w2)
    return g * a + (1 - g) * b
```

```cpp
#include <hip/hip_runtime.h>
#include <hip/hip_bf16.h>
#include <stdint.h>

typedef __attribute__((ext_vector_type(2))) __bf16 bf16x2;
typedef __attribute__((ext_vector_type(4))) unsigned u32x4;
typedef __attribute__((ext_vector_type(2))) unsigned u32x2;
typedef __attribute__((ext_vector_type(4))) float f32x4;
typedef __attribute__((ext_vector_type(2))) float f32x2;
typedef __amdgpu_buffer_rsrc_t rsrc_t;

constexpr int NB = 8, DM = 640, NH = 32, SK = 4096, DH = 128, WIN = 1024, FF = 1728;
constexpr int NFS = FF / 64;
constexpr int NCONS = DM / 32;
#define RING 16
constexpr int R = RING;
constexpr int AUX_NT = 2, AUX_SC1 = 16;
constexpr unsigned SENT = 0xFFFFFFFFu;
constexpr unsigned SPIN_CAP = 1u << 22;
constexpr unsigned WS_EPOCH = 0;
constexpr unsigned WS_READY = 4096;
constexpr unsigned WS_MAIL = 8192;
constexpr unsigned WS_BYTES = WS_MAIL + NB * NCONS * NH * 128;

__device__ __forceinline__ rsrc_t mkrsrc(const void* p, unsigned bytes) {
  return __builtin_amdgcn_make_buffer_rsrc(const_cast<void*>(p), 0, bytes, 0x00020000);
}
template <int AUX> __device__ __forceinline__ u32x4 ld16(rsrc_t r, unsigned voff, unsigned soff) {
  return __builtin_amdgcn_raw_buffer_load_b128(r, voff, soff, AUX);
}
template <int AUX> __device__ __forceinline__ u32x2 ld8(rsrc_t r, unsigned voff, unsigned soff) {
  return __builtin_amdgcn_raw_buffer_load_b64(r, voff, soff, AUX);
}
template <int AUX> __device__ __forceinline__ unsigned ld4(rsrc_t r, unsigned voff, unsigned soff) {
  return __builtin_amdgcn_raw_buffer_load_b32(r, voff, soff, AUX);
}
__device__ __forceinline__ float dot2(unsigned a, unsigned b, float acc) {
  return __builtin_amdgcn_fdot2_f32_bf16(__builtin_bit_cast(bf16x2, a), __builtin_bit_cast(bf16x2, b), acc, false);
}
__device__ __forceinline__ unsigned cvtpk(float lo, float hi) {
  bf16x2 v = {(__bf16)lo, (__bf16)hi};
  return __builtin_bit_cast(unsigned, v);
}
__device__ __forceinline__ unsigned plo(unsigned a, unsigned b) { return __builtin_amdgcn_perm(b, a, 0x05040100u); }
__device__ __forceinline__ unsigned phi(unsigned a, unsigned b) { return __builtin_amdgcn_perm(b, a, 0x07060302u); }
template <int CTRL> __device__ __forceinline__ float dpp(float x) {
  return __builtin_bit_cast(float, __builtin_amdgcn_mov_dpp(__builtin_bit_cast(int, x), CTRL, 0xf, 0xf, true));
}
constexpr int XOR1 = 0xB1, XOR2 = 0x4E, XOR7 = 0x141, ROR8 = 0x128;
__device__ __forceinline__ float xrow_sum(float x) {
  auto s = __builtin_amdgcn_permlane16_swap(__float_as_uint(x), __float_as_uint(x), false, false);
  x = __uint_as_float(s[0]) + __uint_as_float(s[1]);
  auto t = __builtin_amdgcn_permlane32_swap(__float_as_uint(x), __float_as_uint(x), false, false);
  return __uint_as_float(t[0]) + __uint_as_float(t[1]);
}
__device__ __forceinline__ float xrow_max(float x) {
  auto s = __builtin_amdgcn_permlane16_swap(__float_as_uint(x), __float_as_uint(x), false, false);
  x = fmaxf(__uint_as_float(s[0]), __uint_as_float(s[1]));
  auto t = __builtin_amdgcn_permlane32_swap(__float_as_uint(x), __float_as_uint(x), false, false);
  return fmaxf(__uint_as_float(t[0]), __uint_as_float(t[1]));
}
__device__ __forceinline__ float xrow_red4(float a, float b, float c, float d) {
  auto s = __builtin_amdgcn_permlane16_swap(__float_as_uint(a), __float_as_uint(b), false, false);
  auto t = __builtin_amdgcn_permlane16_swap(__float_as_uint(c), __float_as_uint(d), false, false);
  const float x = __uint_as_float(s[0]) + __uint_as_float(s[1]), y = __uint_as_float(t[0]) + __uint_as_float(t[1]);
  auto u = __builtin_amdgcn_permlane32_swap(__float_as_uint(x), __float_as_uint(y), false, false);
  return __uint_as_float(u[0]) + __uint_as_float(u[1]);
}
__device__ __forceinline__ f32x2 xrow_red8(const float* a) {
  return f32x2{xrow_red4(a[0], a[2], a[4], a[6]), xrow_red4(a[1], a[3], a[5], a[7])};
}
template <int L0> __device__ __forceinline__ unsigned row_pick(unsigned x) {
  int y = __builtin_amdgcn_mov_dpp((int)x, 0x150 + L0, 0xf, 0xf, true);
  y = __builtin_amdgcn_update_dpp(y, (int)x, 0x150 + L0 + 2, 0x2, 0xf, false);
  y = __builtin_amdgcn_update_dpp(y, (int)x, 0x150 + L0 + 4, 0x4, 0xf, false);
  y = __builtin_amdgcn_update_dpp(y, (int)x, 0x150 + L0 + 6, 0x8, 0xf, false);
  return (unsigned)y;
}
#define KEEP(x) asm volatile("" : "+v"(x))
#define KEEP8(a) asm volatile("" : "+v"(a[0]), "+v"(a[1]), "+v"(a[2]), "+v"(a[3]), "+v"(a[4]), "+v"(a[5]), "+v"(a[6]), "+v"(a[7]))
__device__ __forceinline__ void pair_fma(const u32x4& A, const u32x4& Bv, unsigned c, float* acc) {
#pragma unroll
  for (int d = 0; d < 4; ++d) {
    acc[2 * d] = dot2(plo(A[d], Bv[d]), c, acc[2 * d]);
    acc[2 * d + 1] = dot2(phi(A[d], Bv[d]), c, acc[2 * d + 1]);
  }
}
__device__ __forceinline__ void lds_barrier() {
  asm volatile("s_waitcnt lgkmcnt(0)\n\ts_barrier" ::: "memory");
}
#define PIN() __builtin_amdgcn_sched_barrier(0)

struct Params {
  const uint16_t* x; const uint16_t* k; const uint16_t* v; const uint16_t* wq; const uint16_t* wo;
  const uint16_t* w1; const uint16_t* w3; const uint16_t* w2; const uint16_t* gw;
  uint16_t* out; unsigned char* ws;
};

__global__ void __launch_bounds__(512, 2) gated_mix_decode_kernel(Params p) {
  __shared__ __attribute__((aligned(16))) float s_q[8][DH];
  __shared__ __attribute__((aligned(16))) float s_gu[8][128];
  __shared__ __attribute__((aligned(16))) float s_red[8][DM];
  __shared__ __attribute__((aligned(16))) float s_g[8];
  __shared__ __attribute__((aligned(16))) float s_ml[16];

  const int tid = threadIdx.x, lane = tid & 63, r = lane >> 4, c = lane & 15;
  const int w = __builtin_amdgcn_readfirstlane(tid >> 6);
  const int bid = blockIdx.x;
  const int h = (bid & 7) + 8 * ((bid >> 3) & 3), b = bid >> 5;
  const bool has_ffn = h < NFS, is_cons = h < NCONS;

  const rsrc_t rws = mkrsrc(p.ws, WS_BYTES);
  const rsrc_t rx = mkrsrc(p.x + b * DM, DM * 2), rgw = mkrsrc(p.gw, DM * 2);
  const size_t kvoff = ((size_t)(b * NH + h) * SK + (SK - WIN)) * DH;
  const rsrc_t rk = mkrsrc(p.k + kvoff, WIN * DH * 2), rv = mkrsrc(p.v + kvoff, WIN * DH * 2);
  const rsrc_t rwq = mkrsrc(p.wq, DM * NH * DH * 2), rwo = mkrsrc(p.wo + (size_t)h * DH * DM, DH * DM * 2);
  const int fs = has_ffn ? h : 0;
  const rsrc_t rw1 = mkrsrc(p.w1, has_ffn ? DM * FF * 2 : 0), rw3 = mkrsrc(p.w3, has_ffn ? DM * FF * 2 : 0);
  const rsrc_t rw2 = mkrsrc(p.w2 + (size_t)fs * 64 * DM, has_ffn ? 64 * DM * 2 : 0);
  const rsrc_t rws0 = mkrsrc(p.ws, w == 0 ? WS_BYTES : 0), rwsc = mkrsrc(p.ws, (w == 0 && is_cons) ? WS_BYTES : 0);

  u32x2 ep = ld8<AUX_SC1>(rws0, 0, WS_EPOCH + b * 256);
  {
    const u32x4 sv = {SENT, SENT, SENT, SENT};
#pragma unroll
    for (int i = 0; i < 4; ++i)
      __builtin_amdgcn_raw_buffer_store_b128(sv, rwsc, lane * 16 + i * 1024, WS_MAIL + (b * NCONS + h) * 4096, AUX_SC1);
  }
  PIN();
  u32x2 xq[5], gq[5];
  const unsigned xoff = (80 * w + 20 * r) * 2;
#pragma unroll
  for (int i = 0; i < 5; ++i) xq[i] = ld8<0>(rx, xoff + 8 * i, 0);
  u32x4 wb[20];
  {
    const unsigned voff = (unsigned)(80 * w + 20 * r) * (NH * DH * 2) + h * 256 + c * 16;
#pragma unroll
    for (int i = 0; i < 20; ++i) { wb[i] = ld16<0>(rwq, voff, i * (NH * DH * 2)); PIN(); }
  }
#pragma unroll
  for (int i = 0; i < 5; ++i) gq[i] = ld8<0>(rgw, xoff + 8 * i, 0);
  unsigned xf[5];
  const int r8 = lane >> 3, c8 = lane & 7;
#pragma unroll
  for (int i = 0; i < 5; ++i) xf[i] = ld4<0>(rx, (80 * w + 10 * r8) * 2 + 4 * i, 0);
  PIN();
  u32x4 S[64];
  const unsigned kvv = w * 1024 + lane * 16;
#define ISSUE(t) do { if ((t) < 32) S[t] = ld16<AUX_NT>(rk, kvv, (t) * 8192); else if ((t) < 64) S[t] = ld16<AUX_NT>(rv, kvv, ((t) - 32) * 8192); } while (0)
#pragma unroll
  for (int t = 0; t < R; ++t) { ISSUE(t); PIN(); }
  PIN();
  {
    float a[8] = {0.f, 0.f, 0.f, 0.f, 0.f, 0.f, 0.f, 0.f};
#pragma unroll
    for (int i = 0; i < 10; ++i) pair_fma(wb[2 * i], wb[2 * i + 1], xq[i >> 1][i & 1], a);
    float gp = 0.f;
#pragma unroll
    for (int i = 0; i < 10; ++i) gp = dot2(xq[i >> 1][i & 1], gq[i >> 1][i & 1], gp);
    KEEP8(a); KEEP(gp);
    PIN();
    {
      const unsigned voff = (unsigned)(80 * w + 10 * r8) * (FF * 2) + fs * 128 + c8 * 16;
#pragma unroll
      for (int i = 0; i < 10; ++i) { wb[i] = ld16<0>(rw1, voff, i * (FF * 2)); wb[10 + i] = ld16<0>(rw3, voff, i * (FF * 2)); }
    }
    PIN();
    *reinterpret_cast<f32x2*>(&s_q[w][8 * c + 2 * r]) = xrow_red8(a);
    s_g[w] = xrow_sum(gp);
  }
  lds_barrier();
  KEEP(ep);
  const unsigned long long T = (((unsigned long long)(unsigned)__builtin_amdgcn_readfirstlane(ep[1]) << 32) | (unsigned)__builtin_amdgcn_readfirstlane(ep[0])) + 1ull;
  {
    const u32x2 tv = {(unsigned)T, (unsigned)(T >> 32)};
    __builtin_amdgcn_raw_buffer_store_b64(tv, rwsc, lane == 0 ? h * 8 : WS_BYTES, WS_READY + b * 256, AUX_SC1);
  }
  unsigned qp[4];
  float g;
  {
    f32x4 q0 = *reinterpret_cast<const f32x4*>(&s_q[2 * r][8 * c]) + *reinterpret_cast<const f32x4*>(&s_q[2 * r + 1][8 * c]);
    f32x4 q1 = *reinterpret_cast<const f32x4*>(&s_q[2 * r][8 * c + 4]) + *reinterpret_cast<const f32x4*>(&s_q[2 * r + 1][8 * c + 4]);
#pragma unroll
    for (int k = 0; k < 4; ++k) { q0[k] = xrow_sum(q0[k]); q1[k] = xrow_sum(q1[k]); }
    qp[0] = cvtpk(q0[0], q0[1]); qp[1] = cvtpk(q0[2], q0[3]); qp[2] = cvtpk(q1[0], q1[1]); qp[3] = cvtpk(q1[2], q1[3]);
    const f32x4 g0 = *reinterpret_cast<const f32x4*>(&s_g[0]), g1 = *reinterpret_cast<const f32x4*>(&s_g[4]);
    const float z = ((g0[0] + g0[1]) + (g0[2] + g0[3])) + ((g1[0] + g1[1]) + (g1[2] + g1[3]));
    g = 1.f / (1.f + __expf(-z));
  }
  PIN();
  constexpr float C2 = 0.08838834764831845f * 1.4426950408889634f;
  float sc[32];
  u32x4 w2b[10];
#pragma unroll
  for (int t = 0; t < 32; ++t) {
    float d = dot2(S[t][0], qp[0], 0.f);
    d = dot2(S[t][1], qp[1], d); d = dot2(S[t][2], qp[2], d); d = dot2(S[t][3], qp[3], d);
    KEEP(d);
    PIN();
    ISSUE(t + R);
    PIN();
    d += dpp<XOR1>(d); d += dpp<XOR2>(d); d += dpp<XOR7>(d); d += dpp<ROR8>(d);
    sc[t] = d * C2;
    KEEP(sc[t]);
    if (t == (R < 24 ? R : 24)) {
      float a[8] = {0.f, 0.f, 0.f, 0.f, 0.f, 0.f, 0.f, 0.f}, u[8] = {0.f, 0.f, 0.f, 0.f, 0.f, 0.f, 0.f, 0.f};
#pragma unroll
      for (int i = 0; i < 5; ++i) { pair_fma(wb[2 * i], wb[2 * i + 1], xf[i], a); pair_fma(wb[10 + 2 * i], wb[11 + 2 * i], xf[i], u); }
      KEEP8(a); KEEP8(u);
      PIN();
#pragma unroll
      for (int i = 0; i < 8; ++i) w2b[i] = ld16<0>(rw2, lane * 16, (8 * w + i) * (DM * 2));
#pragma unroll
      for (int i = 0; i < 2; ++i) w2b[8 + i] = ld16<0>(rw2, (2 * r + i) * (DM * 2) + 1024 + c * 16, 8 * w * (DM * 2));
      PIN();
      const f32x2 za = xrow_red8(a), zu = xrow_red8(u);
      const bool hb = lane & 8;
      s_gu[w][8 * c8 + r8] = (hb ? za[1] : za[0]) + dpp<ROR8>(hb ? za[0] : za[1]);
      s_gu[w][64 + 8 * c8 + r8] = (hb ? zu[1] : zu[0]) + dpp<ROR8>(hb ? zu[0] : zu[1]);
      PIN();
    }
  }
  float mw = sc[0];
#pragma unroll
  for (int t = 1; t < 32; ++t) mw = fmaxf(mw, sc[t]);
  mw = xrow_max(mw);
  float lw = 0.f;
#pragma unroll
  for (int t = 0; t < 32; ++t) { sc[t] = __builtin_amdgcn_exp2f(sc[t] - mw); lw += sc[t]; }
  lw = xrow_sum(lw);
  s_ml[w] = mw; s_ml[8 + w] = lw;
  lds_barrier();
  unsigned pp[16];
  {
    const f32x4 m0 = *reinterpret_cast<const f32x4*>(&s_ml[0]), m1 = *reinterpret_cast<const f32x4*>(&s_ml[4]);
    const f32x4 l0 = *reinterpret_cast<const f32x4*>(&s_ml[8]), l1 = *reinterpret_cast<const f32x4*>(&s_ml[12]);
    const float M = fmaxf(fmaxf(fmaxf(m0[0], m0[1]), fmaxf(m0[2], m0[3])), fmaxf(fmaxf(m1[0], m1[1]), fmaxf(m1[2], m1[3])));
    float L = 0.f;
#pragma unroll
    for (int k = 0; k < 4; ++k) L += l0[k] * __builtin_amdgcn_exp2f(m0[k] - M) + l1[k] * __builtin_amdgcn_exp2f(m1[k] - M);
    const float f = __builtin_amdgcn_exp2f(mw - M) / L;
#pragma unroll
    for (int t = 0; t < 16; ++t) { pp[t] = cvtpk(sc[2 * t] * f, sc[2 * t + 1] * f); KEEP(pp[t]); }
  }
  float om[8] = {0.f, 0.f, 0.f, 0.f, 0.f, 0.f, 0.f, 0.f}, orr[8] = {0.f, 0.f, 0.f, 0.f, 0.f, 0.f, 0.f, 0.f};
  {
    float ga = s_gu[r8][8 * w + c8], up = s_gu[r8][64 + 8 * w + c8];
    ga += dpp<ROR8>(ga); ga = xrow_sum(ga); up += dpp<ROR8>(up); up = xrow_sum(up);
    const float hid = ga / (1.f + __expf(-ga)) * up;
    const unsigned hp = cvtpk(hid, dpp<XOR1>(hid));
    unsigned hs[4];
#pragma unroll
    for (int u = 0; u < 4; ++u) hs[u] = __builtin_amdgcn_readlane(hp, 2 * u);
#pragma unroll
    for (int u = 0; u < 4; ++u) pair_fma(w2b[2 * u], w2b[2 * u + 1], hs[u], om);
    pair_fma(w2b[8], w2b[9], row_pick<0>(hp), orr);
#pragma unroll
    for (int k = 0; k < 8; ++k) { om[k] *= 1.f - g; orr[k] *= 1.f - g; }
    KEEP8(om); KEEP8(orr);
  }
  PIN();
#pragma unroll
  for (int i = 0; i < 16; ++i) wb[i] = ld16<0>(rwo, lane * 16, (16 * w + i) * (DM * 2));
#pragma unroll
  for (int i = 0; i < 4; ++i) wb[16 + i] = ld16<0>(rwo, (2 * r + (i & 1) + 8 * (i >> 1)) * (DM * 2) + 1024 + c * 16, 16 * w * (DM * 2));
  const unsigned rdoff = (lane < NCONS ? lane : 0) * 8;
  u32x2 rdy = ld8<AUX_SC1>(rws0, rdoff, WS_READY + b * 256);
  PIN();
  float acc[8] = {0.f, 0.f, 0.f, 0.f, 0.f, 0.f, 0.f, 0.f};
  unsigned wp[80];
#pragma unroll
  for (int t = 0; t < 16; ++t) {
    pair_fma(S[32 + 2 * t], S[33 + 2 * t], pp[t], acc);
    KEEP8(acc);
    PIN();
    ISSUE(32 + 2 * t + R); ISSUE(33 + 2 * t + R);
    PIN();
    if (t == 13) {
#pragma unroll
      for (int u = 0; u < 10; ++u)
#pragma unroll
        for (int d = 0; d < 4; ++d) {
          wp[8 * u + 2 * d] = plo(wb[2 * u][d], wb[2 * u + 1][d]); wp[8 * u + 2 * d + 1] = phi(wb[2 * u][d], wb[2 * u + 1][d]);
          KEEP(wp[8 * u + 2 * d]); KEEP(wp[8 * u + 2 * d + 1]);
        }
      PIN();
    }
  }
  *reinterpret_cast<f32x2*>(&s_q[w][8 * c + 2 * r]) = xrow_red8(acc);
  lds_barrier();
  {
    const float o = xrow_sum(s_q[2 * r][16 * w + c] + s_q[2 * r + 1][16 * w + c]);
    const unsigned op = cvtpk(o, dpp<XOR1>(o));
    unsigned os[8];
#pragma unroll
    for (int u = 0; u < 8; ++u) os[u] = __builtin_amdgcn_readlane(op, 2 * u);
    float am[8] = {0.f, 0.f, 0.f, 0.f, 0.f, 0.f, 0.f, 0.f}, ar[8] = {0.f, 0.f, 0.f, 0.f, 0.f, 0.f, 0.f, 0.f};
    const unsigned c0 = row_pick<0>(op), c1 = row_pick<8>(op);
#pragma unroll
    for (int k = 0; k < 8; ++k) { ar[k] = dot2(wp[64 + k], c0, ar[k]); ar[k] = dot2(wp[72 + k], c1, ar[k]); }
#pragma unroll
    for (int u = 0; u < 8; ++u)
#pragma unroll
      for (int k = 0; k < 8; ++k) am[k] = dot2(wp[8 * u + k], os[u], am[k]);
#pragma unroll
    for (int k = 0; k < 8; ++k) { am[k] = fmaf(g, am[k], om[k]); ar[k] = fmaf(g, ar[k], orr[k]); }
    *reinterpret_cast<f32x4*>(&s_red[w][8 * lane]) = f32x4{am[0], am[1], am[2], am[3]};
    *reinterpret_cast<f32x4*>(&s_red[w][8 * lane + 4]) = f32x4{am[4], am[5], am[6], am[7]};
    *reinterpret_cast<f32x2*>(&s_red[w][512 + 8 * c + 2 * r]) = xrow_red8(ar);
  }
  if (w == 0) {
#pragma nounroll
    for (unsigned spins = 0; spins < SPIN_CAP; ++spins) {
      if (__all((((unsigned long long)rdy[1] << 32) | rdy[0]) == T)) break;
      __builtin_amdgcn_s_sleep(4);
      rdy = ld8<AUX_SC1>(rws, rdoff, WS_READY + b * 256);
    }
  }
  lds_barrier();
  if (tid < DM / 4) {
    f32x4 s = *reinterpret_cast<const f32x4*>(&s_red[0][4 * tid]);
#pragma unroll
    for (int k = 1; k < 8; ++k) s += *reinterpret_cast<const f32x4*>(&s_red[k][4 * tid]);
    u32x4 sv = __builtin_bit_cast(u32x4, s);
#pragma unroll
    for (int k = 0; k < 4; ++k) sv[k] = sv[k] == SENT ? 0x7FC00000u : sv[k];
    __builtin_amdgcn_raw_buffer_store_b128(sv, rws, (tid >> 3) * 4096 + (tid & 7) * 16, WS_MAIL + b * NCONS * 4096 + h * 128, AUX_SC1);
  }
  if (w == 3 && is_cons) {
    u32x4 m[4];
#pragma nounroll
    for (unsigned spins = 0; spins < SPIN_CAP; ++spins) {
#pragma unroll
      for (int i = 0; i < 4; ++i) m[i] = ld16<AUX_SC1>(rws, lane * 16 + i * 1024, WS_MAIL + (b * NCONS + h) * 4096);
      bool ok = true;
#pragma unroll
      for (int i = 0; i < 4; ++i)
#pragma unroll
        for (int k = 0; k < 4; ++k) ok = ok && (m[i][k] != SENT);
      if (__all(ok)) break;
      __builtin_amdgcn_s_sleep(2);
    }
    f32x4 s = (__builtin_bit_cast(f32x4, m[0]) + __builtin_bit_cast(f32x4, m[1])) + (__builtin_bit_cast(f32x4, m[2]) + __builtin_bit_cast(f32x4, m[3]));
#pragma unroll
    for (int k = 0; k < 4; ++k) { s[k] += dpp<ROR8>(s[k]); s[k] = xrow_sum(s[k]); }
    if (lane < 8) {
      const u32x2 ov = {cvtpk(s[0], s[1]), cvtpk(s[2], s[3])};
      *reinterpret_cast<u32x2*>(p.out + b * DM + h * 32 + lane * 4) = ov;
    }
    if (h == 0) {
      const u32x2 e = ld8<AUX_SC1>(rws, 0, WS_READY + b * 256);
      if (lane == 0) __builtin_amdgcn_raw_buffer_store_b64(e, rws, 0, WS_EPOCH + b * 256, AUX_SC1);
    }
  }
}

extern "C" void kernel_launch(void* const* d_in, const int* in_sizes, int n_in, void* d_out, int out_size, void* d_ws,
                              size_t ws_size, hipStream_t stream) {
  Params p;
  p.x = (const uint16_t*)d_in[0]; p.k = (const uint16_t*)d_in[1]; p.v = (const uint16_t*)d_in[2];
  p.wq = (const uint16_t*)d_in[3]; p.wo = (const uint16_t*)d_in[4];
  p.w1 = (const uint16_t*)d_in[6]; p.w3 = (const uint16_t*)d_in[7]; p.w2 = (const uint16_t*)d_in[8];
  p.gw = (const uint16_t*)d_in[9];
  p.out = (uint16_t*)d_out; p.ws = (unsigned char*)d_ws;
  hipLaunchKernelGGL(gated_mix_decode_kernel, dim3(NB * NH), dim3(512), 0, stream, p);
}
```

```cpp
#include <hip/hip_runtime.h>
#include <hip/hip_bf16.h>
#include <stdint.h>

typedef __attribute__((ext_vector_type(2))) __bf16 bf16x2;
typedef __attribute__((ext_vector_type(4))) unsigned u32x4;
typedef __attribute__((ext_vector_type(2))) unsigned u32x2;
typedef __attribute__((ext_vector_type(4))) float f32x4;
typedef __attribute__((ext_vector_type(2))) float f32x2;
typedef __amdgpu_buffer_rsrc_t rsrc_t;

constexpr int NB = 8, DM = 640, NH = 32, SK = 4096, DH = 128, WIN = 1024, FF = 1728;
constexpr int NFS = FF / 64;
constexpr int NCONS = DM / 32;
#define RING 16
#define WAUX 2
#define KAUX 2
#define NOW 0
constexpr int R = RING;
constexpr int AUX_NT = 2, AUX_SC1 = 16;
constexpr unsigned SENT = 0xFFFFFFFFu;
constexpr unsigned SPIN_CAP = 1u << 20;
constexpr unsigned WS_EPOCH = 0;
constexpr unsigned WS_READY = 4096;
constexpr unsigned WS_MAIL = 8192;
constexpr unsigned WS_BYTES = WS_MAIL + NB * NCONS * NH * 128;

__device__ __forceinline__ rsrc_t mkrsrc(const void* p, unsigned bytes) {
  return __builtin_amdgcn_make_buffer_rsrc(const_cast<void*>(p), 0, bytes, 0x00020000);
}
template <int AUX> __device__ __forceinline__ u32x4 ld16(rsrc_t r, unsigned voff, unsigned soff) {
  return __builtin_amdgcn_raw_buffer_load_b128(r, voff, soff, AUX);
}
template <int AUX> __device__ __forceinline__ u32x2 ld8(rsrc_t r, unsigned voff, unsigned soff) {
  return __builtin_amdgcn_raw_buffer_load_b64(r, voff, soff, AUX);
}
template <int AUX> __device__ __forceinline__ unsigned ld4(rsrc_t r, unsigned voff, unsigned soff) {
  return __builtin_amdgcn_raw_buffer_load_b32(r, voff, soff, AUX);
}
__device__ __forceinline__ float dot2(unsigned a, unsigned b, float acc) {
  return __builtin_amdgcn_fdot2_f32_bf16(__builtin_bit_cast(bf16x2, a), __builtin_bit_cast(bf16x2, b), acc, false);
}
__device__ __forceinline__ unsigned cvtpk(float lo, float hi) {
  bf16x2 v = {(__bf16)lo, (__bf16)hi};
  return __builtin_bit_cast(unsigned, v);
}
__device__ __forceinline__ unsigned plo(unsigned a, unsigned b) { return __builtin_amdgcn_perm(b, a, 0x05040100u); }
__device__ __forceinline__ unsigned phi(unsigned a, unsigned b) { return __builtin_amdgcn_perm(b, a, 0x07060302u); }
template <int CTRL> __device__ __forceinline__ float dpp(float x) {
  return __builtin_bit_cast(float, __builtin_amdgcn_mov_dpp(__builtin_bit_cast(int, x), CTRL, 0xf, 0xf, true));
}
constexpr int XOR1 = 0xB1, XOR2 = 0x4E, XOR7 = 0x141, ROR8 = 0x128;
__device__ __forceinline__ float xrow_sum(float x) {
  auto s = __builtin_amdgcn_permlane16_swap(__float_as_uint(x), __float_as_uint(x), false, false);
  x = __uint_as_float(s[0]) + __uint_as_float(s[1]);
  auto t = __builtin_amdgcn_permlane32_swap(__float_as_uint(x), __float_as_uint(x), false, false);
  return __uint_as_float(t[0]) + __uint_as_float(t[1]);
}
__device__ __forceinline__ float xrow_max(float x) {
  auto s = __builtin_amdgcn_permlane16_swap(__float_as_uint(x), __float_as_uint(x), false, false);
  x = fmaxf(__uint_as_float(s[0]), __uint_as_float(s[1]));
  auto t = __builtin_amdgcn_permlane32_swap(__float_as_uint(x), __float_as_uint(x), false, false);
  return fmaxf(__uint_as_float(t[0]), __uint_as_float(t[1]));
}
__device__ __forceinline__ float xrow_red4(float a, float b, float c, float d) {
  auto s = __builtin_amdgcn_permlane16_swap(__float_as_uint(a), __float_as_uint(b), false, false);
  auto t = __builtin_amdgcn_permlane16_swap(__float_as_uint(c), __float_as_uint(d), false, false);
  const float x = __uint_as_float(s[0]) + __uint_as_float(s[1]), y = __uint_as_float(t[0]) + __uint_as_float(t[1]);
  auto u = __builtin_amdgcn_permlane32_swap(__float_as_uint(x), __float_as_uint(y), false, false);
  return __uint_as_float(u[0]) + __uint_as_float(u[1]);
}
__device__ __forceinline__ f32x2 xrow_red8(const float* a) {
  return f32x2{xrow_red4(a[0], a[2], a[4], a[6]), xrow_red4(a[1], a[3], a[5], a[7])};
}
template <int L0> __device__ __forceinline__ unsigned row_pick(unsigned x) {
  int y = __builtin_amdgcn_mov_dpp((int)x, 0x150 + L0, 0xf, 0xf, true);
  y = __builtin_amdgcn_update_dpp(y, (int)x, 0x150 + L0 + 2, 0x2, 0xf, false);
  y = __builtin_amdgcn_update_dpp(y, (int)x, 0x150 + L0 + 4, 0x4, 0xf, false);
  y = __builtin_amdgcn_update_dpp(y, (int)x, 0x150 + L0 + 6, 0x8, 0xf, false);
  return (unsigned)y;
}
#define KEEP(x) asm volatile("" : "+v"(x))
#define KEEP8(a) asm volatile("" : "+v"(a[0]), "+v"(a[1]), "+v"(a[2]), "+v"(a[3]), "+v"(a[4]), "+v"(a[5]), "+v"(a[6]), "+v"(a[7]))
__device__ __forceinline__ void pair_fma(const u32x4& A, const u32x4& Bv, unsigned c, float* acc) {
#pragma unroll
  for (int d = 0; d < 4; ++d) {
    acc[2 * d] = dot2(plo(A[d], Bv[d]), c, acc[2 * d]);
    acc[2 * d + 1] = dot2(phi(A[d], Bv[d]), c, acc[2 * d + 1]);
  }
}
__device__ __forceinline__ void lds_barrier() {
  asm volatile("s_waitcnt lgkmcnt(0)\n\ts_barrier" ::: "memory");
}
#define PIN() __builtin_amdgcn_sched_barrier(0)
#define ST(k, cond)
#define STV(k)

struct Params {
  const uint16_t* x; const uint16_t* k; const uint16_t* v; const uint16_t* wq; const uint16_t* wo;
  const uint16_t* w1; const uint16_t* w3; const uint16_t* w2; const uint16_t* gw;
  uint16_t* out; unsigned char* ws;
};

__global__ void __launch_bounds__(512, 2) gated_mix_decode_kernel(Params p) {
  __shared__ __attribute__((aligned(16))) float s_q[8][DH];
  __shared__ __attribute__((aligned(16))) float s_gu[8][128];
  __shared__ __attribute__((aligned(16))) float s_red[8][DM];
  __shared__ __attribute__((aligned(16))) float s_g[8];
  __shared__ __attribute__((aligned(16))) float s_ml[16];

  const int tid = threadIdx.x, lane = tid & 63, r = lane >> 4, c = lane & 15;
  const int w = __builtin_amdgcn_readfirstlane(tid >> 6);
  const int bid = blockIdx.x;
  const int h = (bid & 7) + 8 * ((bid >> 3) & 3), b = bid >> 5;
  const bool has_ffn = (NOW & 4) ? false : h < NFS, is_cons = h < NCONS;

  const rsrc_t rws = mkrsrc(p.ws, WS_BYTES);
  const rsrc_t rx = mkrsrc(p.x + b * DM, DM * 2), rgw = mkrsrc(p.gw, DM * 2);
  const size_t kvoff = ((size_t)(b * NH + h) * SK + (SK - WIN)) * DH;
  const rsrc_t rk = mkrsrc(p.k + kvoff, WIN * DH * 2), rv = mkrsrc(p.v + kvoff, WIN * DH * 2);
  const rsrc_t rwq = mkrsrc(p.wq, (NOW & 1) ? 0 : DM * NH * DH * 2), rwo = mkrsrc(p.wo + (size_t)h * DH * DM, (NOW & 2) ? 0 : DH * DM * 2);
  const int fs = has_ffn ? h : 0;
  const rsrc_t rw1 = mkrsrc(p.w1, has_ffn ? DM * FF * 2 : 0), rw3 = mkrsrc(p.w3, has_ffn ? DM * FF * 2 : 0);
  const rsrc_t rw2 = mkrsrc(p.w2 + (size_t)fs * 64 * DM, has_ffn ? 64 * DM * 2 : 0);
  const rsrc_t rws0 = mkrsrc(p.ws, w == 0 ? WS_BYTES : 0), rwsc = mkrsrc(p.ws, (w == 0 && is_cons) ? WS_BYTES : 0);

  STV(0);
  u32x2 ep = ld8<AUX_SC1>(rws0, 0, WS_EPOCH + b * 256);
  {
    const u32x4 sv = {SENT, SENT, SENT, SENT};
#pragma unroll
    for (int i = 0; i < 4; ++i)
      __builtin_amdgcn_raw_buffer_store_b128(sv, rwsc, lane * 16 + i * 1024, WS_MAIL + (b * NCONS + h) * 4096, AUX_SC1);
  }
  PIN();
  u32x2 xq[5], gq[5];
  const unsigned xoff = (80 * w + 20 * r) * 2;
#pragma unroll
  for (int i = 0; i < 5; ++i) xq[i] = ld8<0>(rx, xoff + 8 * i, 0);
  u32x4 wb[20];
  {
    const unsigned voff = (unsigned)(80 * w + 20 * r) * (NH * DH * 2) + h * 256 + c * 16;
#pragma unroll
    for (int i = 0; i < 20; ++i) { wb[i] = ld16<WAUX>(rwq, voff, i * (NH * DH * 2)); PIN(); }
  }
#pragma unroll
  for (int i = 0; i < 5; ++i) gq[i] = ld8<0>(rgw, xoff + 8 * i, 0);
  unsigned xf[5];
  const int r8 = lane >> 3, c8 = lane & 7;
#pragma unroll
  for (int i = 0; i < 5; ++i) xf[i] = ld4<0>(rx, (80 * w + 10 * r8) * 2 + 4 * i, 0);
  PIN();
  u32x4 S[64];
  const unsigned kvv = w * 1024 + lane * 16;
#define ISSUE(t) do { if ((t) < 32) S[t] = ld16<KAUX>(rk, kvv, (t) * 8192); else if ((t) < 64) S[t] = ld16<KAUX>(rv, kvv, ((t) - 32) * 8192); } while (0)
#pragma unroll
  for (int t = 0; t < R; ++t) { ISSUE(t); PIN(); }
  PIN();
  {
    float a[8] = {0.f, 0.f, 0.f, 0.f, 0.f, 0.f, 0.f, 0.f};
#pragma unroll
    for (int i = 0; i < 10; ++i) pair_fma(wb[2 * i], wb[2 * i + 1], xq[i >> 1][i & 1], a);
    float gp = 0.f;
#pragma unroll
    for (int i = 0; i < 10; ++i) gp = dot2(xq[i >> 1][i & 1], gq[i >> 1][i & 1], gp);
    KEEP8(a); KEEP(gp);
    PIN();
    {
      const unsigned voff = (unsigned)(80 * w + 10 * r8) * (FF * 2) + fs * 128 + c8 * 16;
#pragma unroll
      for (int i = 0; i < 10; ++i) { wb[i] = ld16<WAUX>(rw1, voff, i * (FF * 2)); wb[10 + i] = ld16<WAUX>(rw3, voff, i * (FF * 2)); }
    }
    PIN();
    *reinterpret_cast<f32x2*>(&s_q[w][8 * c + 2 * r]) = xrow_red8(a);
    s_g[w] = xrow_sum(gp);
  }
  STV(1);
  lds_barrier();
  KEEP(ep);
  const unsigned long long T = (((unsigned long long)(unsigned)__builtin_amdgcn_readfirstlane(ep[1]) << 32) | (unsigned)__builtin_amdgcn_readfirstlane(ep[0])) + 1ull;
  {
    const u32x2 tv = {(unsigned)T, (unsigned)(T >> 32)};
    __builtin_amdgcn_raw_buffer_store_b64(tv, rwsc, lane == 0 ? h * 8 : WS_BYTES, WS_READY + b * 256, AUX_SC1);
  }
  unsigned qp[4];
  float g;
  {
    f32x4 q0 = *reinterpret_cast<const f32x4*>(&s_q[2 * r][8 * c]) + *reinterpret_cast<const f32x4*>(&s_q[2 * r + 1][8 * c]);
    f32x4 q1 = *reinterpret_cast<const f32x4*>(&s_q[2 * r][8 * c + 4]) + *reinterpret_cast<const f32x4*>(&s_q[2 * r + 1][8 * c + 4]);
#pragma unroll
    for (int k = 0; k < 4; ++k) { q0[k] = xrow_sum(q0[k]); q1[k] = xrow_sum(q1[k]); }
    qp[0] = cvtpk(q0[0], q0[1]); qp[1] = cvtpk(q0[2], q0[3]); qp[2] = cvtpk(q1[0], q1[1]); qp[3] = cvtpk(q1[2], q1[3]);
    const f32x4 g0 = *reinterpret_cast<const f32x4*>(&s_g[0]), g1 = *reinterpret_cast<const f32x4*>(&s_g[4]);
    const float z = ((g0[0] + g0[1]) + (g0[2] + g0[3])) + ((g1[0] + g1[1]) + (g1[2] + g1[3]));
    g = 1.f / (1.f + __expf(-z));
  }
  PIN();
  constexpr float C2 = 0.08838834764831845f * 1.4426950408889634f;
  float sc[32];
  u32x4 w2b[10];
#pragma unroll
  for (int t = 0; t < 32; ++t) {
    float d = dot2(S[t][0], qp[0], 0.f);
    d = dot2(S[t][1], qp[1], d); d = dot2(S[t][2], qp[2], d); d = dot2(S[t][3], qp[3], d);
    KEEP(d);
    PIN();
    ISSUE(t + R);
    PIN();
    d += dpp<XOR1>(d); d += dpp<XOR2>(d); d += dpp<XOR7>(d); d += dpp<ROR8>(d);
    sc[t] = d * C2;
    KEEP(sc[t]);
    if (t == (R < 24 ? R : 24)) {
      float a[8] = {0.f, 0.f, 0.f, 0.f, 0.f, 0.f, 0.f, 0.f}, u[8] = {0.f, 0.f, 0.f, 0.f, 0.f, 0.f, 0.f, 0.f};
#pragma unroll
      for (int i = 0; i < 5; ++i) { pair_fma(wb[2 * i], wb[2 * i + 1], xf[i], a); pair_fma(wb[10 + 2 * i], wb[11 + 2 * i], xf[i], u); }
      KEEP8(a); KEEP8(u);
      PIN();
#pragma unroll
      for (int i = 0; i < 8; ++i) w2b[i] = ld16<WAUX>(rw2, lane * 16, (8 * w + i) * (DM * 2));
#pragma unroll
      for (int i = 0; i < 2; ++i) w2b[8 + i] = ld16<WAUX>(rw2, (2 * r + i) * (DM * 2) + 1024 + c * 16, 8 * w * (DM * 2));
      PIN();
      const f32x2 za = xrow_red8(a), zu = xrow_red8(u);
      const bool hb = lane & 8;
      s_gu[w][8 * c8 + r8] = (hb ? za[1] : za[0]) + dpp<ROR8>(hb ? za[0] : za[1]);
      s_gu[w][64 + 8 * c8 + r8] = (hb ? zu[1] : zu[0]) + dpp<ROR8>(hb ? zu[0] : zu[1]);
      PIN();
    }
  }
  float mw = sc[0];
#pragma unroll
  for (int t = 1; t < 32; ++t) mw = fmaxf(mw, sc[t]);
  mw = xrow_max(mw);
  float lw = 0.f;
#pragma unroll
  for (int t = 0; t < 32; ++t) { sc[t] = __builtin_amdgcn_exp2f(sc[t] - mw); lw += sc[t]; }
  lw = xrow_sum(lw);
  s_ml[w] = mw; s_ml[8 + w] = lw;
  lds_barrier();
  STV(2);
  unsigned pp[16];
  {
    const f32x4 m0 = *reinterpret_cast<const f32x4*>(&s_ml[0]), m1 = *reinterpret_cast<const f32x4*>(&s_ml[4]);
    const f32x4 l0 = *reinterpret_cast<const f32x4*>(&s_ml[8]), l1 = *reinterpret_cast<const f32x4*>(&s_ml[12]);
    const float M = fmaxf(fmaxf(fmaxf(m0[0], m0[1]), fmaxf(m0[2], m0[3])), fmaxf(fmaxf(m1[0], m1[1]), fmaxf(m1[2], m1[3])));
    float L = 0.f;
#pragma unroll
    for (int k = 0; k < 4; ++k) L += l0[k] * __builtin_amdgcn_exp2f(m0[k] - M) + l1[k] * __builtin_amdgcn_exp2f(m1[k] - M);
    const float f = __builtin_amdgcn_exp2f(mw - M) / L;
#pragma unroll
    for (int t = 0; t < 16; ++t) { pp[t] = cvtpk(sc[2 * t] * f, sc[2 * t + 1] * f); KEEP(pp[t]); }
  }
  float om[8] = {0.f, 0.f, 0.f, 0.f, 0.f, 0.f, 0.f, 0.f}, orr[8] = {0.f, 0.f, 0.f, 0.f, 0.f, 0.f, 0.f, 0.f};
  {
    float ga = s_gu[r8][8 * w + c8], up = s_gu[r8][64 + 8 * w + c8];
    ga += dpp<ROR8>(ga); ga = xrow_sum(ga); up += dpp<ROR8>(up); up = xrow_sum(up);
    const float hid = ga / (1.f + __expf(-ga)) * up;
    const unsigned hp = cvtpk(hid, dpp<XOR1>(hid));
    unsigned hs[4];
#pragma unroll
    for (int u = 0; u < 4; ++u) hs[u] = __builtin_amdgcn_readlane(hp, 2 * u);
#pragma unroll
    for (int u = 0; u < 4; ++u) pair_fma(w2b[2 * u], w2b[2 * u + 1], hs[u], om);
    pair_fma(w2b[8], w2b[9], row_pick<0>(hp), orr);
#pragma unroll
    for (int k = 0; k < 8; ++k) { om[k] *= 1.f - g; orr[k] *= 1.f - g; }
    KEEP8(om); KEEP8(orr);
  }
  PIN();
#pragma unroll
  for (int i = 0; i < 16; ++i) wb[i] = ld16<WAUX>(rwo, lane * 16, (16 * w + i) * (DM * 2));
#pragma unroll
  for (int i = 0; i < 4; ++i) wb[16 + i] = ld16<WAUX>(rwo, (2 * r + (i & 1) + 8 * (i >> 1)) * (DM * 2) + 1024 + c * 16, 16 * w * (DM * 2));
  const unsigned rdoff = (lane < NCONS ? lane : 0) * 8;
  u32x2 rdy = ld8<AUX_SC1>(rws0, rdoff, WS_READY + b * 256);
  PIN();
  float acc[8] = {0.f, 0.f, 0.f, 0.f, 0.f, 0.f, 0.f, 0.f};
  unsigned wp[80];
#pragma unroll
  for (int t = 0; t < 16; ++t) {
    pair_fma(S[32 + 2 * t], S[33 + 2 * t], pp[t], acc);
    KEEP8(acc);
    PIN();
    ISSUE(32 + 2 * t + R); ISSUE(33 + 2 * t + R);
    PIN();
    if (t == 13) {
#pragma unroll
      for (int u = 0; u < 10; ++u)
#pragma unroll
        for (int d = 0; d < 4; ++d) {
          wp[8 * u + 2 * d] = plo(wb[2 * u][d], wb[2 * u + 1][d]); wp[8 * u + 2 * d + 1] = phi(wb[2 * u][d], wb[2 * u + 1][d]);
          KEEP(wp[8 * u + 2 * d]); KEEP(wp[8 * u + 2 * d + 1]);
        }
      PIN();
    }
  }
  *reinterpret_cast<f32x2*>(&s_q[w][8 * c + 2 * r]) = xrow_red8(acc);
  STV(3);
  lds_barrier();
  STV(4);
  {
    const float o = xrow_sum(s_q[2 * r][16 * w + c] + s_q[2 * r + 1][16 * w + c]);
    const unsigned op = cvtpk(o, dpp<XOR1>(o));
    unsigned os[8];
#pragma unroll
    for (int u = 0; u < 8; ++u) os[u] = __builtin_amdgcn_readlane(op, 2 * u);
    float am[8] = {0.f, 0.f, 0.f, 0.f, 0.f, 0.f, 0.f, 0.f}, ar[8] = {0.f, 0.f, 0.f, 0.f, 0.f, 0.f, 0.f, 0.f};
    const unsigned c0 = row_pick<0>(op), c1 = row_pick<8>(op);
#pragma unroll
    for (int k = 0; k < 8; ++k) { ar[k] = dot2(wp[64 + k], c0, ar[k]); ar[k] = dot2(wp[72 + k], c1, ar[k]); }
#pragma unroll
    for (int u = 0; u < 8; ++u)
#pragma unroll
      for (int k = 0; k < 8; ++k) am[k] = dot2(wp[8 * u + k], os[u], am[k]);
#pragma unroll
    for (int k = 0; k < 8; ++k) { am[k] = fmaf(g, am[k], om[k]); ar[k] = fmaf(g, ar[k], orr[k]); }
    *reinterpret_cast<f32x4*>(&s_red[w][8 * lane]) = f32x4{am[0], am[1], am[2], am[3]};
    *reinterpret_cast<f32x4*>(&s_red[w][8 * lane + 4]) = f32x4{am[4], am[5], am[6], am[7]};
    *reinterpret_cast<f32x2*>(&s_red[w][512 + 8 * c + 2 * r]) = xrow_red8(ar);
  }
  if (w == 0) {
#pragma nounroll
    for (unsigned spins = 0; spins < SPIN_CAP; ++spins) {
      if (__all((((unsigned long long)rdy[1] << 32) | rdy[0]) == T)) break;
      __builtin_amdgcn_s_sleep(4);
      rdy = ld8<AUX_SC1>(rws, rdoff, WS_READY + b * 256);
    }
  }
  lds_barrier();
  STV(5);
  if (tid < DM / 4) {
    f32x4 s = *reinterpret_cast<const f32x4*>(&s_red[0][4 * tid]);
#pragma unroll
    for (int k = 1; k < 8; ++k) s += *reinterpret_cast<const f32x4*>(&s_red[k][4 * tid]);
    u32x4 sv = __builtin_bit_cast(u32x4, s);
#pragma unroll
    for (int k = 0; k < 4; ++k) sv[k] = sv[k] == SENT ? 0x7FC00000u : sv[k];
    __builtin_amdgcn_raw_buffer_store_b128(sv, rws, (tid >> 3) * 4096 + (tid & 7) * 16, WS_MAIL + b * NCONS * 4096 + h * 128, AUX_SC1);
  }
  if (w == 3 && is_cons) {
    u32x4 m[4];
#pragma nounroll
    for (unsigned spins = 0; spins < SPIN_CAP; ++spins) {
#pragma unroll
      for (int i = 0; i < 4; ++i) m[i] = ld16<AUX_SC1>(rws, lane * 16 + i * 1024, WS_MAIL + (b * NCONS + h) * 4096);
      bool ok = true;
#pragma unroll
      for (int i = 0; i < 4; ++i)
#pragma unroll
        for (int k = 0; k < 4; ++k) ok = ok && (m[i][k] != SENT);
      if (__all(ok)) break;
      __builtin_amdgcn_s_sleep(2);
    }
    f32x4 s = (__builtin_bit_cast(f32x4, m[0]) + __builtin_bit_cast(f32x4, m[1])) + (__builtin_bit_cast(f32x4, m[2]) + __builtin_bit_cast(f32x4, m[3]));
#pragma unroll
    for (int k = 0; k < 4; ++k) { s[k] += dpp<ROR8>(s[k]); s[k] = xrow_sum(s[k]); }
    if (lane < 8) {
      const u32x2 ov = {cvtpk(s[0], s[1]), cvtpk(s[2], s[3])};
      *reinterpret_cast<u32x2*>(p.out + b * DM + h * 32 + lane * 4) = ov;
    }
    ST(6, true);
    if (h == 0) {
      const u32x2 e = ld8<AUX_SC1>(rws, 0, WS_READY + b * 256);
      if (lane == 0) __builtin_amdgcn_raw_buffer_store_b64(e, rws, 0, WS_EPOCH + b * 256, AUX_SC1);
    }
  }
}

extern "C" void kernel_launch(void* const* d_in, const int* in_sizes, int n_in, void* d_out, int out_size, void* d_ws,
                              size_t ws_size, hipStream_t stream) {
  Params p;
  p.x = (const uint16_t*)d_in[0]; p.k = (const uint16_t*)d_in[1]; p.v = (const uint16_t*)d_in[2];
  p.wq = (const uint16_t*)d_in[3]; p.wo = (const uint16_t*)d_in[4];
  p.w1 = (const uint16_t*)d_in[6]; p.w3 = (const uint16_t*)d_in[7]; p.w2 = (const uint16_t*)d_in[8];
  p.gw = (const uint16_t*)d_in[9];
  p.out = (uint16_t*)d_out; p.ws = (unsigned char*)d_ws;
  hipLaunchKernelGGL(gated_mix_decode_kernel, dim3(NB * NH), dim3(512), 0, stream, p);
}
```

```cpp
#include <hip/hip_runtime.h>
#include <hip/hip_bf16.h>
#include <stdint.h>
#define STAMP
#include <vector>
#include <algorithm>
#include <cstdio>

typedef __attribute__((ext_vector_type(2))) __bf16 bf16x2;
typedef __attribute__((ext_vector_type(4))) unsigned u32x4;
typedef __attribute__((ext_vector_type(2))) unsigned u32x2;
typedef __attribute__((ext_vector_type(4))) float f32x4;
typedef __attribute__((ext_vector_type(2))) float f32x2;
typedef __amdgpu_buffer_rsrc_t rsrc_t;

constexpr int NB = 8, DM = 640, NH = 32, SK = 4096, DH = 128, WIN = 1024, FF = 1728;
constexpr int NFS = FF / 64;
constexpr int NCONS = DM / 32;
#define RING 16
#define WAUX 2
#define KAUX 2
#define NOW 0
constexpr int R = RING;
constexpr int AUX_NT = 2, AUX_SC1 = 16;
constexpr unsigned SENT = 0xFFFFFFFFu;
constexpr unsigned SPIN_CAP = 1u << 20;
constexpr unsigned WS_EPOCH = 0;
constexpr unsigned WS_READY = 4096;
constexpr unsigned WS_MAIL = 8192;
constexpr unsigned WS_BYTES = WS_MAIL + NB * NCONS * NH * 128;

__device__ __forceinline__ rsrc_t mkrsrc(const void* p, unsigned bytes) {
  return __builtin_amdgcn_make_buffer_rsrc(const_cast<void*>(p), 0, bytes, 0x00020000);
}
template <int AUX> __device__ __forceinline__ u32x4 ld16(rsrc_t r, unsigned voff, unsigned soff) {
  return __builtin_amdgcn_raw_buffer_load_b128(r, voff, soff, AUX);
}
template <int AUX> __device__ __forceinline__ u32x2 ld8(rsrc_t r, unsigned voff, unsigned soff) {
  return __builtin_amdgcn_raw_buffer_load_b64(r, voff, soff, AUX);
}
template <int AUX> __device__ __forceinline__ unsigned ld4(rsrc_t r, unsigned voff, unsigned soff) {
  return __builtin_amdgcn_raw_buffer_load_b32(r, voff, soff, AUX);
}
__device__ __forceinline__ float dot2(unsigned a, unsigned b, float acc) {
  return __builtin_amdgcn_fdot2_f32_bf16(__builtin_bit_cast(bf16x2, a), __builtin_bit_cast(bf16x2, b), acc, false);
}
__device__ __forceinline__ unsigned cvtpk(float lo, float hi) {
  bf16x2 v = {(__bf16)lo, (__bf16)hi};
  return __builtin_bit_cast(unsigned, v);
}
__device__ __forceinline__ unsigned plo(unsigned a, unsigned b) { return __builtin_amdgcn_perm(b, a, 0x05040100u); }
__device__ __forceinline__ unsigned phi(unsigned a, unsigned b) { return __builtin_amdgcn_perm(b, a, 0x07060302u); }
template <int CTRL> __device__ __forceinline__ float dpp(float x) {
  return __builtin_bit_cast(float, __builtin_amdgcn_mov_dpp(__builtin_bit_cast(int, x), CTRL, 0xf, 0xf, true));
}
constexpr int XOR1 = 0xB1, XOR2 = 0x4E, XOR7 = 0x141, ROR8 = 0x128;
__device__ __forceinline__ float xrow_sum(float x) {
  auto s = __builtin_amdgcn_permlane16_swap(__float_as_uint(x), __float_as_uint(x), false, false);
  x = __uint_as_float(s[0]) + __uint_as_float(s[1]);
  auto t = __builtin_amdgcn_permlane32_swap(__float_as_uint(x), __float_as_uint(x), false, false);
  return __uint_as_float(t[0]) + __uint_as_float(t[1]);
}
__device__ __forceinline__ float xrow_max(float x) {
  auto s = __builtin_amdgcn_permlane16_swap(__float_as_uint(x), __float_as_uint(x), false, false);
  x = fmaxf(__uint_as_float(s[0]), __uint_as_float(s[1]));
  auto t = __builtin_amdgcn_permlane32_swap(__float_as_uint(x), __float_as_uint(x), false, false);
  return fmaxf(__uint_as_float(t[0]), __uint_as_float(t[1]));
}
__device__ __forceinline__ float xrow_red4(float a, float b, float c, float d) {
  auto s = __builtin_amdgcn_permlane16_swap(__float_as_uint(a), __float_as_uint(b), false, false);
  auto t = __builtin_amdgcn_permlane16_swap(__float_as_uint(c), __float_as_uint(d), false, false);
  const float x = __uint_as_float(s[0]) + __uint_as_float(s[1]), y = __uint_as_float(t[0]) + __uint_as_float(t[1]);
  auto u = __builtin_amdgcn_permlane32_swap(__float_as_uint(x), __float_as_uint(y), false, false);
  return __uint_as_float(u[0]) + __uint_as_float(u[1]);
}
__device__ __forceinline__ f32x2 xrow_red8(const float* a) {
  return f32x2{xrow_red4(a[0], a[2], a[4], a[6]), xrow_red4(a[1], a[3], a[5], a[7])};
}
template <int L0> __device__ __forceinline__ unsigned row_pick(unsigned x) {
  int y = __builtin_amdgcn_mov_dpp((int)x, 0x150 + L0, 0xf, 0xf, true);
  y = __builtin_amdgcn_update_dpp(y, (int)x, 0x150 + L0 + 2, 0x2, 0xf, false);
  y = __builtin_amdgcn_update_dpp(y, (int)x, 0x150 + L0 + 4, 0x4, 0xf, false);
  y = __builtin_amdgcn_update_dpp(y, (int)x, 0x150 + L0 + 6, 0x8, 0xf, false);
  return (unsigned)y;
}
#define KEEP(x) asm volatile("" : "+v"(x))
#define KEEP8(a) asm volatile("" : "+v"(a[0]), "+v"(a[1]), "+v"(a[2]), "+v"(a[3]), "+v"(a[4]), "+v"(a[5]), "+v"(a[6]), "+v"(a[7]))
__device__ __forceinline__ void pair_fma(const u32x4& A, const u32x4& Bv, unsigned c, float* acc) {
#pragma unroll
  for (int d = 0; d < 4; ++d) {
    acc[2 * d] = dot2(plo(A[d], Bv[d]), c, acc[2 * d]);
    acc[2 * d + 1] = dot2(phi(A[d], Bv[d]), c, acc[2 * d + 1]);
  }
}
__device__ __forceinline__ void lds_barrier() {
  asm volatile("s_waitcnt lgkmcnt(0)\n\ts_barrier" ::: "memory");
}
#define PIN() __builtin_amdgcn_sched_barrier(0)
#define ST(k, cond) do { PIN(); unsigned long long t_ = __builtin_amdgcn_s_memrealtime(); if (cond) { if (lane == 0) *(unsigned long long*)(p.ws + (64 << 20) + (blockIdx.x * 16 + (k)) * 8) = t_; } PIN(); } while (0)
#define STV(k) do { PIN(); stv[k] = __builtin_amdgcn_s_memrealtime(); PIN(); } while (0)

struct Params {
  const uint16_t* x; const uint16_t* k; const uint16_t* v; const uint16_t* wq; const uint16_t* wo;
  const uint16_t* w1; const uint16_t* w3; const uint16_t* w2; const uint16_t* gw;
  uint16_t* out; unsigned char* ws;
};

__global__ void __launch_bounds__(512, 2) gated_mix_decode_kernel(Params p) {
  __shared__ __attribute__((aligned(16))) float s_q[8][DH];
  __shared__ __attribute__((aligned(16))) float s_gu[8][128];
  __shared__ __attribute__((aligned(16))) float s_red[8][DM];
  __shared__ __attribute__((aligned(16))) float s_g[8];
  __shared__ __attribute__((aligned(16))) float s_ml[16];

  const int tid = threadIdx.x, lane = tid & 63, r = lane >> 4, c = lane & 15;
  const int w = __builtin_amdgcn_readfirstlane(tid >> 6);
  const int bid = blockIdx.x;
  const int h = (bid & 7) + 8 * ((bid >> 3) & 3), b = bid >> 5;
  const bool has_ffn = (NOW & 4) ? false : h < NFS, is_cons = h < NCONS;

  const rsrc_t rws = mkrsrc(p.ws, WS_BYTES);
  const rsrc_t rx = mkrsrc(p.x + b * DM, DM * 2), rgw = mkrsrc(p.gw, DM * 2);
  const size_t kvoff = ((size_t)(b * NH + h) * SK + (SK - WIN)) * DH;
  const rsrc_t rk = mkrsrc(p.k + kvoff, WIN * DH * 2), rv = mkrsrc(p.v + kvoff, WIN * DH * 2);
  const rsrc_t rwq = mkrsrc(p.wq, (NOW & 1) ? 0 : DM * NH * DH * 2), rwo = mkrsrc(p.wo + (size_t)h * DH * DM, (NOW & 2) ? 0 : DH * DM * 2);
  const int fs = has_ffn ? h : 0;
  const rsrc_t rw1 = mkrsrc(p.w1, has_ffn ? DM * FF * 2 : 0), rw3 = mkrsrc(p.w3, has_ffn ? DM * FF * 2 : 0);
  const rsrc_t rw2 = mkrsrc(p.w2 + (size_t)fs * 64 * DM, has_ffn ? 64 * DM * 2 : 0);
  const rsrc_t rws0 = mkrsrc(p.ws, w == 0 ? WS_BYTES : 0), rwsc = mkrsrc(p.ws, (w == 0 && is_cons) ? WS_BYTES : 0);

  unsigned long long stv[8];
  STV(0);
  u32x2 ep = ld8<AUX_SC1>(rws0, 0, WS_EPOCH + b * 256);
  {
    const u32x4 sv = {SENT, SENT, SENT, SENT};
#pragma unroll
    for (int i = 0; i < 4; ++i)
      __builtin_amdgcn_raw_buffer_store_b128(sv, rwsc, lane * 16 + i * 1024, WS_MAIL + (b * NCONS + h) * 4096, AUX_SC1);
  }
  PIN();
  u32x2 xq[5], gq[5];
  const unsigned xoff = (80 * w + 20 * r) * 2;
#pragma unroll
  for (int i = 0; i < 5; ++i) xq[i] = ld8<0>(rx, xoff + 8 * i, 0);
  u32x4 wb[20];
  {
    const unsigned voff = (unsigned)(80 * w + 20 * r) * (NH * DH * 2) + h * 256 + c * 16;
#pragma unroll
    for (int i = 0; i < 20; ++i) { wb[i] = ld16<WAUX>(rwq, voff, i * (NH * DH * 2)); PIN(); }
  }
#pragma unroll
  for (int i = 0; i < 5; ++i) gq[i] = ld8<0>(rgw, xoff + 8 * i, 0);
  unsigned xf[5];
  const int r8 = lane >> 3, c8 = lane & 7;
#pragma unroll
  for (int i = 0; i < 5; ++i) xf[i] = ld4<0>(rx, (80 * w + 10 * r8) * 2 + 4 * i, 0);
  PIN();
  u32x4 S[64];
  const unsigned kvv = w * 1024 + lane * 16;
#define ISSUE(t) do { if ((t) < 32) S[t] = ld16<KAUX>(rk, kvv, (t) * 8192); else if ((t) < 64) S[t] = ld16<KAUX>(rv, kvv, ((t) - 32) * 8192); } while (0)
#pragma unroll
  for (int t = 0; t < R; ++t) { ISSUE(t); PIN(); }
  PIN();
  {
    float a[8] = {0.f, 0.f, 0.f, 0.f, 0.f, 0.f, 0.f, 0.f};
#pragma unroll
    for (int i = 0; i < 10; ++i) pair_fma(wb[2 * i], wb[2 * i + 1], xq[i >> 1][i & 1], a);
    float gp = 0.f;
#pragma unroll
    for (int i = 0; i < 10; ++i) gp = dot2(xq[i >> 1][i & 1], gq[i >> 1][i & 1], gp);
    KEEP8(a); KEEP(gp);
    PIN();
    {
      const unsigned voff = (unsigned)(80 * w + 10 * r8) * (FF * 2) + fs * 128 + c8 * 16;
#pragma unroll
      for (int i = 0; i < 10; ++i) { wb[i] = ld16<WAUX>(rw1, voff, i * (FF * 2)); wb[10 + i] = ld16<WAUX>(rw3, voff, i * (FF * 2)); }
    }
    PIN();
    *reinterpret_cast<f32x2*>(&s_q[w][8 * c + 2 * r]) = xrow_red8(a);
    s_g[w] = xrow_sum(gp);
  }
  STV(1);
  lds_barrier();
  KEEP(ep);
  const unsigned long long T = (((unsigned long long)(unsigned)__builtin_amdgcn_readfirstlane(ep[1]) << 32) | (unsigned)__builtin_amdgcn_readfirstlane(ep[0])) + 1ull;
  {
    const u32x2 tv = {(unsigned)T, (unsigned)(T >> 32)};
    __builtin_amdgcn_raw_buffer_store_b64(tv, rwsc, lane == 0 ? h * 8 : WS_BYTES, WS_READY + b * 256, AUX_SC1);
  }
  unsigned qp[4];
  float g;
  {
    f32x4 q0 = *reinterpret_cast<const f32x4*>(&s_q[2 * r][8 * c]) + *reinterpret_cast<const f32x4*>(&s_q[2 * r + 1][8 * c]);
    f32x4 q1 = *reinterpret_cast<const f32x4*>(&s_q[2 * r][8 * c + 4]) + *reinterpret_cast<const f32x4*>(&s_q[2 * r + 1][8 * c + 4]);
#pragma unroll
    for (int k = 0; k < 4; ++k) { q0[k] = xrow_sum(q0[k]); q1[k] = xrow_sum(q1[k]); }
    qp[0] = cvtpk(q0[0], q0[1]); qp[1] = cvtpk(q0[2], q0[3]); qp[2] = cvtpk(q1[0], q1[1]); qp[3] = cvtpk(q1[2], q1[3]);
    const f32x4 g0 = *reinterpret_cast<const f32x4*>(&s_g[0]), g1 = *reinterpret_cast<const f32x4*>(&s_g[4]);
    const float z = ((g0[0] + g0[1]) + (g0[2] + g0[3])) + ((g1[0] + g1[1]) + (g1[2] + g1[3]));
    g = 1.f / (1.f + __expf(-z));
  }
  PIN();
  constexpr float C2 = 0.08838834764831845f * 1.4426950408889634f;
  float sc[32];
  u32x4 w2b[10];
#pragma unroll
  for (int t = 0; t < 32; ++t) {
    float d = dot2(S[t][0], qp[0], 0.f);
    d = dot2(S[t][1], qp[1], d); d = dot2(S[t][2], qp[2], d); d = dot2(S[t][3], qp[3], d);
    KEEP(d);
    PIN();
    ISSUE(t + R);
    PIN();
    d += dpp<XOR1>(d); d += dpp<XOR2>(d); d += dpp<XOR7>(d); d += dpp<ROR8>(d);
    sc[t] = d * C2;
    KEEP(sc[t]);
    if (t == (R < 24 ? R : 24)) {
      float a[8] = {0.f, 0.f, 0.f, 0.f, 0.f, 0.f, 0.f, 0.f}, u[8] = {0.f, 0.f, 0.f, 0.f, 0.f, 0.f, 0.f, 0.f};
#pragma unroll
      for (int i = 0; i < 5; ++i) { pair_fma(wb[2 * i], wb[2 * i + 1], xf[i], a); pair_fma(wb[10 + 2 * i], wb[11 + 2 * i], xf[i], u); }
      KEEP8(a); KEEP8(u);
      PIN();
#pragma unroll
      for (int i = 0; i < 8; ++i) w2b[i] = ld16<WAUX>(rw2, lane * 16, (8 * w + i) * (DM * 2));
#pragma unroll
      for (int i = 0; i < 2; ++i) w2b[8 + i] = ld16<WAUX>(rw2, (2 * r + i) * (DM * 2) + 1024 + c * 16, 8 * w * (DM * 2));
      PIN();
      const f32x2 za = xrow_red8(a), zu = xrow_red8(u);
      const bool hb = lane & 8;
      s_gu[w][8 * c8 + r8] = (hb ? za[1] : za[0]) + dpp<ROR8>(hb ? za[0] : za[1]);
      s_gu[w][64 + 8 * c8 + r8] = (hb ? zu[1] : zu[0]) + dpp<ROR8>(hb ? zu[0] : zu[1]);
      PIN();
    }
  }
  float mw = sc[0];
#pragma unroll
  for (int t = 1; t < 32; ++t) mw = fmaxf(mw, sc[t]);
  mw = xrow_max(mw);
  float lw = 0.f;
#pragma unroll
  for (int t = 0; t < 32; ++t) { sc[t] = __builtin_amdgcn_exp2f(sc[t] - mw); lw += sc[t]; }
  lw = xrow_sum(lw);
  s_ml[w] = mw; s_ml[8 + w] = lw;
  lds_barrier();
  STV(2);
  unsigned pp[16];
  {
    const f32x4 m0 = *reinterpret_cast<const f32x4*>(&s_ml[0]), m1 = *reinterpret_cast<const f32x4*>(&s_ml[4]);
    const f32x4 l0 = *reinterpret_cast<const f32x4*>(&s_ml[8]), l1 = *reinterpret_cast<const f32x4*>(&s_ml[12]);
    const float M = fmaxf(fmaxf(fmaxf(m0[0], m0[1]), fmaxf(m0[2], m0[3])), fmaxf(fmaxf(m1[0], m1[1]), fmaxf(m1[2], m1[3])));
    float L = 0.f;
#pragma unroll
    for (int k = 0; k < 4; ++k) L += l0[k] * __builtin_amdgcn_exp2f(m0[k] - M) + l1[k] * __builtin_amdgcn_exp2f(m1[k] - M);
    const float f = __builtin_amdgcn_exp2f(mw - M) / L;
#pragma unroll
    for (int t = 0; t < 16; ++t) { pp[t] = cvtpk(sc[2 * t] * f, sc[2 * t + 1] * f); KEEP(pp[t]); }
  }
  float om[8] = {0.f, 0.f, 0.f, 0.f, 0.f, 0.f, 0.f, 0.f}, orr[8] = {0.f, 0.f, 0.f, 0.f, 0.f, 0.f, 0.f, 0.f};
  {
    float ga = s_gu[r8][8 * w + c8], up = s_gu[r8][64 + 8 * w + c8];
    ga += dpp<ROR8>(ga); ga = xrow_sum(ga); up += dpp<ROR8>(up); up = xrow_sum(up);
    const float hid = ga / (1.f + __expf(-ga)) * up;
    const unsigned hp = cvtpk(hid, dpp<XOR1>(hid));
    unsigned hs[4];
#pragma unroll
    for (int u = 0; u < 4; ++u) hs[u] = __builtin_amdgcn_readlane(hp, 2 * u);
#pragma unroll
    for (int u = 0; u < 4; ++u) pair_fma(w2b[2 * u], w2b[2 * u + 1], hs[u], om);
    pair_fma(w2b[8], w2b[9], row_pick<0>(hp), orr);
#pragma unroll
    for (int k = 0; k < 8; ++k) { om[k] *= 1.f - g; orr[k] *= 1.f - g; }
    KEEP8(om); KEEP8(orr);
  }
  PIN();
#pragma unroll
  for (int i = 0; i < 16; ++i) wb[i] = ld16<WAUX>(rwo, lane * 16, (16 * w + i) * (DM * 2));
#pragma unroll
  for (int i = 0; i < 4; ++i) wb[16 + i] = ld16<WAUX>(rwo, (2 * r + (i & 1) + 8 * (i >> 1)) * (DM * 2) + 1024 + c * 16, 16 * w * (DM * 2));
  const unsigned rdoff = (lane < NCONS ? lane : 0) * 8;
  u32x2 rdy = ld8<AUX_SC1>(rws0, rdoff, WS_READY + b * 256);
  PIN();
  float acc[8] = {0.f, 0.f, 0.f, 0.f, 0.f, 0.f, 0.f, 0.f};
  unsigned wp[80];
#pragma unroll
  for (int t = 0; t < 16; ++t) {
    pair_fma(S[32 + 2 * t], S[33 + 2 * t], pp[t], acc);
    KEEP8(acc);
    PIN();
    ISSUE(32 + 2 * t + R); ISSUE(33 + 2 * t + R);
    PIN();
    if (t == 13) {
#pragma unroll
      for (int u = 0; u < 10; ++u)
#pragma unroll
        for (int d = 0; d < 4; ++d) {
          wp[8 * u + 2 * d] = plo(wb[2 * u][d], wb[2 * u + 1][d]); wp[8 * u + 2 * d + 1] = phi(wb[2 * u][d], wb[2 * u + 1][d]);
          KEEP(wp[8 * u + 2 * d]); KEEP(wp[8 * u + 2 * d + 1]);
        }
      PIN();
    }
  }
  *reinterpret_cast<f32x2*>(&s_q[w][8 * c + 2 * r]) = xrow_red8(acc);
  STV(3);
  lds_barrier();
  STV(4);
  {
    const float o = xrow_sum(s_q[2 * r][16 * w + c] + s_q[2 * r + 1][16 * w + c]);
    const unsigned op = cvtpk(o, dpp<XOR1>(o));
    unsigned os[8];
#pragma unroll
    for (int u = 0; u < 8; ++u) os[u] = __builtin_amdgcn_readlane(op, 2 * u);
    float am[8] = {0.f, 0.f, 0.f, 0.f, 0.f, 0.f, 0.f, 0.f}, ar[8] = {0.f, 0.f, 0.f, 0.f, 0.f, 0.f, 0.f, 0.f};
    const unsigned c0 = row_pick<0>(op), c1 = row_pick<8>(op);
#pragma unroll
    for (int k = 0; k < 8; ++k) { ar[k] = dot2(wp[64 + k], c0, ar[k]); ar[k] = dot2(wp[72 + k], c1, ar[k]); }
#pragma unroll
    for (int u = 0; u < 8; ++u)
#pragma unroll
      for (int k = 0; k < 8; ++k) am[k] = dot2(wp[8 * u + k], os[u], am[k]);
#pragma unroll
    for (int k = 0; k < 8; ++k) { am[k] = fmaf(g, am[k], om[k]); ar[k] = fmaf(g, ar[k], orr[k]); }
    *reinterpret_cast<f32x4*>(&s_red[w][8 * lane]) = f32x4{am[0], am[1], am[2], am[3]};
    *reinterpret_cast<f32x4*>(&s_red[w][8 * lane + 4]) = f32x4{am[4], am[5], am[6], am[7]};
    *reinterpret_cast<f32x2*>(&s_red[w][512 + 8 * c + 2 * r]) = xrow_red8(ar);
  }
  if (w == 0) {
#pragma nounroll
    for (unsigned spins = 0; spins < SPIN_CAP; ++spins) {
      if (__all((((unsigned long long)rdy[1] << 32) | rdy[0]) == T)) break;
      __builtin_amdgcn_s_sleep(4);
      rdy = ld8<AUX_SC1>(rws, rdoff, WS_READY + b * 256);
    }
  }
  lds_barrier();
  STV(5);
  if (tid < DM / 4) {
    f32x4 s = *reinterpret_cast<const f32x4*>(&s_red[0][4 * tid]);
#pragma unroll
    for (int k = 1; k < 8; ++k) s += *reinterpret_cast<const f32x4*>(&s_red[k][4 * tid]);
    u32x4 sv = __builtin_bit_cast(u32x4, s);
#pragma unroll
    for (int k = 0; k < 4; ++k) sv[k] = sv[k] == SENT ? 0x7FC00000u : sv[k];
    __builtin_amdgcn_raw_buffer_store_b128(sv, rws, (tid >> 3) * 4096 + (tid & 7) * 16, WS_MAIL + b * NCONS * 4096 + h * 128, AUX_SC1);
  }
  if (w == 0 && lane == 0) for (int k = 0; k < 6; ++k) *(unsigned long long*)(p.ws + (64 << 20) + (blockIdx.x * 16 + k) * 8) = stv[k];
  if (w == 3 && is_cons) {
    u32x4 m[4];
#pragma nounroll
    for (unsigned spins = 0; spins < SPIN_CAP; ++spins) {
#pragma unroll
      for (int i = 0; i < 4; ++i) m[i] = ld16<AUX_SC1>(rws, lane * 16 + i * 1024, WS_MAIL + (b * NCONS + h) * 4096);
      bool ok = true;
#pragma unroll
      for (int i = 0; i < 4; ++i)
#pragma unroll
        for (int k = 0; k < 4; ++k) ok = ok && (m[i][k] != SENT);
      if (__all(ok)) break;
      __builtin_amdgcn_s_sleep(2);
    }
    f32x4 s = (__builtin_bit_cast(f32x4, m[0]) + __builtin_bit_cast(f32x4, m[1])) + (__builtin_bit_cast(f32x4, m[2]) + __builtin_bit_cast(f32x4, m[3]));
#pragma unroll
    for (int k = 0; k < 4; ++k) { s[k] += dpp<ROR8>(s[k]); s[k] = xrow_sum(s[k]); }
    if (lane < 8) {
      const u32x2 ov = {cvtpk(s[0], s[1]), cvtpk(s[2], s[3])};
      *reinterpret_cast<u32x2*>(p.out + b * DM + h * 32 + lane * 4) = ov;
    }
    ST(6, true);
    if (h == 0) {
      const u32x2 e = ld8<AUX_SC1>(rws, 0, WS_READY + b * 256);
      if (lane == 0) __builtin_amdgcn_raw_buffer_store_b64(e, rws, 0, WS_EPOCH + b * 256, AUX_SC1);
    }
  }
}

extern "C" void kernel_launch(void* const* d_in, const int* in_sizes, int n_in, void* d_out, int out_size, void* d_ws,
                              size_t ws_size, hipStream_t stream) {
  Params p;
  p.x = (const uint16_t*)d_in[0]; p.k = (const uint16_t*)d_in[1]; p.v = (const uint16_t*)d_in[2];
  p.wq = (const uint16_t*)d_in[3]; p.wo = (const uint16_t*)d_in[4];
  p.w1 = (const uint16_t*)d_in[6]; p.w3 = (const uint16_t*)d_in[7]; p.w2 = (const uint16_t*)d_in[8];
  p.gw = (const uint16_t*)d_in[9];
  p.out = (uint16_t*)d_out; p.ws = (unsigned char*)d_ws;
  hipStreamCaptureStatus cs; hipStreamIsCapturing(stream, &cs);
  if (cs == hipStreamCaptureStatusNone) {
    fprintf(stderr, "ptrs k %p v %p wq %p ws %p out %p\n", d_in[1], d_in[2], d_in[3], d_ws, d_out);
    for (int it = 0; it < 8; ++it) {
      if (it >= 4) hipMemsetAsync(d_ws, 0x5A + it, ws_size, stream);
      hipMemsetAsync((char*)d_ws + (64 << 20), 0, 256 * 16 * 8, stream);
      hipStreamSynchronize(stream);
      hipLaunchKernelGGL(gated_mix_decode_kernel, dim3(NB * NH), dim3(512), 0, stream, p);
      hipStreamSynchronize(stream);
      const int NS = 16; std::vector<unsigned long long> stamps(256 * NS);
      hipMemcpy(stamps.data(), (char*)d_ws + (64 << 20), 256 * NS * 8, hipMemcpyDeviceToHost);
      unsigned long long t0 = ~0ull; for (int w = 0; w < 256; ++w) t0 = std::min(t0, stamps[w * NS]);
      for (int k = 0; k < NS; ++k) { std::vector<double> v; for (int w = 0; w < 256; ++w) if (stamps[w * NS + k]) v.push_back((stamps[w * NS + k] - t0) * 0.01);
        if (v.empty()) continue; std::sort(v.begin(), v.end()); fprintf(stderr, " s%d[%.2f %.2f %.2f]", k, v[0], v[v.size() / 2], v.back()); }
      fprintf(stderr, "\n");
    }
  }
  hipLaunchKernelGGL(gated_mix_decode_kernel, dim3(NB * NH), dim3(512), 0, stream, p);
}
```

```cpp
#include <hip/hip_runtime.h>
#include <hip/hip_bf16.h>
#include <stdint.h>
#define STAMP
#define PADK 0

typedef __attribute__((ext_vector_type(2))) __bf16 bf16x2;
typedef __attribute__((ext_vector_type(4))) unsigned u32x4;
typedef __attribute__((ext_vector_type(2))) unsigned u32x2;
typedef __attribute__((ext_vector_type(4))) float f32x4;
typedef __attribute__((ext_vector_type(2))) float f32x2;
typedef __amdgpu_buffer_rsrc_t rsrc_t;

constexpr int NB = 8, DM = 640, NH = 32, SK = 4096, DH = 128, WIN = 1024, FF = 1728;
constexpr int NFS = FF / 64;
constexpr int NCONS = DM / 32;
#define RING 16
#define WAUX 2
#define KAUX 2
#define NOW 0
constexpr int R = RING;
constexpr int AUX_NT = 2, AUX_SC1 = 16;
constexpr unsigned SENT = 0xFFFFFFFFu;
constexpr unsigned SPIN_CAP = 1u << 20;
constexpr unsigned WS_EPOCH = 0;
constexpr unsigned WS_READY = 4096;
constexpr unsigned WS_MAIL = 8192;
constexpr unsigned WS_BYTES = WS_MAIL + NB * NCONS * NH * 128;

__device__ __forceinline__ rsrc_t mkrsrc(const void* p, unsigned bytes) {
  return __builtin_amdgcn_make_buffer_rsrc(const_cast<void*>(p), 0, bytes, 0x00020000);
}
template <int AUX> __device__ __forceinline__ u32x4 ld16(rsrc_t r, unsigned voff, unsigned soff) {
  return __builtin_amdgcn_raw_buffer_load_b128(r, voff, soff, AUX);
}
template <int AUX> __device__ __forceinline__ u32x2 ld8(rsrc_t r, unsigned voff, unsigned soff) {
  return __builtin_amdgcn_raw_buffer_load_b64(r, voff, soff, AUX);
}
template <int AUX> __device__ __forceinline__ unsigned ld4(rsrc_t r, unsigned voff, unsigned soff) {
  return __builtin_amdgcn_raw_buffer_load_b32(r, voff, soff, AUX);
}
__device__ __forceinline__ float dot2(unsigned a, unsigned b, float acc) {
  return __builtin_amdgcn_fdot2_f32_bf16(__builtin_bit_cast(bf16x2, a), __builtin_bit_cast(bf16x2, b), acc, false);
}
__device__ __forceinline__ unsigned cvtpk(float lo, float hi) {
  bf16x2 v = {(__bf16)lo, (__bf16)hi};
  return __builtin_bit_cast(unsigned, v);
}
__device__ __forceinline__ unsigned plo(unsigned a, unsigned b) { return __builtin_amdgcn_perm(b, a, 0x05040100u); }
__device__ __forceinline__ unsigned phi(unsigned a, unsigned b) { return __builtin_amdgcn_perm(b, a, 0x07060302u); }
template <int CTRL> __device__ __forceinline__ float dpp(float x) {
  return __builtin_bit_cast(float, __builtin_amdgcn_mov_dpp(__builtin_bit_cast(int, x), CTRL, 0xf, 0xf, true));
}
constexpr int XOR1 = 0xB1, XOR2 = 0x4E, XOR7 = 0x141, ROR8 = 0x128;
__device__ __forceinline__ float xrow_sum(float x) {
  auto s = __builtin_amdgcn_permlane16_swap(__float_as_uint(x), __float_as_uint(x), false, false);
  x = __uint_as_float(s[0]) + __uint_as_float(s[1]);
  auto t = __builtin_amdgcn_permlane32_swap(__float_as_uint(x), __float_as_uint(x), false, false);
  return __uint_as_float(t[0]) + __uint_as_float(t[1]);
}
__device__ __forceinline__ float xrow_max(float x) {
  auto s = __builtin_amdgcn_permlane16_swap(__float_as_uint(x), __float_as_uint(x), false, false);
  x = fmaxf(__uint_as_float(s[0]), __uint_as_float(s[1]));
  auto t = __builtin_amdgcn_permlane32_swap(__float_as_uint(x), __float_as_uint(x), false, false);
  return fmaxf(__uint_as_float(t[0]), __uint_as_float(t[1]));
}
__device__ __forceinline__ float xrow_red4(float a, float b, float c, float d) {
  auto s = __builtin_amdgcn_permlane16_swap(__float_as_uint(a), __float_as_uint(b), false, false);
  auto t = __builtin_amdgcn_permlane16_swap(__float_as_uint(c), __float_as_uint(d), false, false);
  const float x = __uint_as_float(s[0]) + __uint_as_float(s[1]), y = __uint_as_float(t[0]) + __uint_as_float(t[1]);
  auto u = __builtin_amdgcn_permlane32_swap(__float_as_uint(x), __float_as_uint(y), false, false);
  return __uint_as_float(u[0]) + __uint_as_float(u[1]);
}
__device__ __forceinline__ f32x2 xrow_red8(const float* a) {
  return f32x2{xrow_red4(a[0], a[2], a[4], a[6]), xrow_red4(a[1], a[3], a[5], a[7])};
}
template <int L0> __device__ __forceinline__ unsigned row_pick(unsigned x) {
  int y = __builtin_amdgcn_mov_dpp((int)x, 0x150 + L0, 0xf, 0xf, true);
  y = __builtin_amdgcn_update_dpp(y, (int)x, 0x150 + L0 + 2, 0x2, 0xf, false);
  y = __builtin_amdgcn_update_dpp(y, (int)x, 0x150 + L0 + 4, 0x4, 0xf, false);
  y = __builtin_amdgcn_update_dpp(y, (int)x, 0x150 + L0 + 6, 0x8, 0xf, false);
  return (unsigned)y;
}
#define KEEP(x) asm volatile("" : "+v"(x))
#define KEEP8(a) asm volatile("" : "+v"(a[0]), "+v"(a[1]), "+v"(a[2]), "+v"(a[3]), "+v"(a[4]), "+v"(a[5]), "+v"(a[6]), "+v"(a[7]))
__device__ __forceinline__ void pair_fma(const u32x4& A, const u32x4& Bv, unsigned c, float* acc) {
#pragma unroll
  for (int d = 0; d < 4; ++d) {
    acc[2 * d] = dot2(plo(A[d], Bv[d]), c, acc[2 * d]);
    acc[2 * d + 1] = dot2(phi(A[d], Bv[d]), c, acc[2 * d + 1]);
  }
}
__device__ __forceinline__ void lds_barrier() {
  asm volatile("s_waitcnt lgkmcnt(0)\n\ts_barrier" ::: "memory");
}
#define PIN() __builtin_amdgcn_sched_barrier(0)
#define ST(k, cond) do { PIN(); unsigned long long t_ = __builtin_amdgcn_s_memrealtime(); if (cond) { if (lane == 0) *(unsigned long long*)(p.ws + (64 << 20) + (blockIdx.x * 16 + (k)) * 8) = t_; } PIN(); } while (0)
#define STV(k) do { PIN(); stv[k] = __builtin_amdgcn_s_memrealtime(); PIN(); } while (0)

struct Params {
  const uint16_t* x; const uint16_t* k; const uint16_t* v; const uint16_t* wq; const uint16_t* wo;
  const uint16_t* w1; const uint16_t* w3; const uint16_t* w2; const uint16_t* gw;
  uint16_t* out; unsigned char* ws;
};

__global__ void __launch_bounds__(512, 2) gated_mix_decode_kernel(Params p) {
  __shared__ __attribute__((aligned(16))) float s_q[8][DH];
  __shared__ __attribute__((aligned(16))) float s_gu[8][128];
  __shared__ __attribute__((aligned(16))) float s_red[8][DM];
  __shared__ __attribute__((aligned(16))) float s_g[8];
  __shared__ __attribute__((aligned(16))) float s_ml[16];

  const int tid = threadIdx.x, lane = tid & 63, r = lane >> 4, c = lane & 15;
  const int w = __builtin_amdgcn_readfirstlane(tid >> 6);
  const int bid = blockIdx.x;
  const int h = (bid & 7) + 8 * ((bid >> 3) & 3), b = bid >> 5;
  const bool has_ffn = (NOW & 4) ? false : h < NFS, is_cons = h < NCONS;

  const rsrc_t rws = mkrsrc(p.ws, WS_BYTES);
  const rsrc_t rx = mkrsrc(p.x + b * DM, DM * 2), rgw = mkrsrc(p.gw, DM * 2);
  const size_t kvoff = ((size_t)(b * NH + h) * SK + (SK - WIN)) * DH;
  const rsrc_t rk = mkrsrc(p.k + kvoff, WIN * DH * 2), rv = mkrsrc(p.v + kvoff, WIN * DH * 2);
  const rsrc_t rwq = mkrsrc(p.wq, (NOW & 1) ? 0 : DM * NH * DH * 2), rwo = mkrsrc(p.wo + (size_t)h * DH * DM, (NOW & 2) ? 0 : DH * DM * 2);
  const int fs = has_ffn ? h : 0;
  const rsrc_t rw1 = mkrsrc(p.w1, has_ffn ? DM * FF * 2 : 0), rw3 = mkrsrc(p.w3, has_ffn ? DM * FF * 2 : 0);
  const rsrc_t rw2 = mkrsrc(p.w2 + (size_t)fs * 64 * DM, has_ffn ? 64 * DM * 2 : 0);
  const rsrc_t rws0 = mkrsrc(p.ws, w == 0 ? WS_BYTES : 0), rwsc = mkrsrc(p.ws, (w == 0 && is_cons) ? WS_BYTES : 0);

  unsigned long long stv[8];
  STV(0);
  u32x2 ep = ld8<AUX_SC1>(rws0, 0, WS_EPOCH + b * 256);
  {
    const u32x4 sv = {SENT, SENT, SENT, SENT};
#pragma unroll
    for (int i = 0; i < 4; ++i)
      __builtin_amdgcn_raw_buffer_store_b128(sv, rwsc, lane * 16 + i * 1024, WS_MAIL + (b * NCONS + h) * 4096, AUX_SC1);
  }
  PIN();
  u32x2 xq[5], gq[5];
  const unsigned xoff = (80 * w + 20 * r) * 2;
#pragma unroll
  for (int i = 0; i < 5; ++i) xq[i] = ld8<0>(rx, xoff + 8 * i, 0);
  u32x4 wb[20];
  {
    const unsigned voff = (unsigned)(80 * w + 20 * r) * (NH * DH * 2) + h * 256 + c * 16;
#pragma unroll
    for (int i = 0; i < 20; ++i) { wb[i] = ld16<WAUX>(rwq, voff, i * (NH * DH * 2)); PIN(); }
  }
#pragma unroll
  for (int i = 0; i < 5; ++i) gq[i] = ld8<0>(rgw, xoff + 8 * i, 0);
  unsigned xf[5];
  const int r8 = lane >> 3, c8 = lane & 7;
#pragma unroll
  for (int i = 0; i < 5; ++i) xf[i] = ld4<0>(rx, (80 * w + 10 * r8) * 2 + 4 * i, 0);
  PIN();
  u32x4 S[64];
  const unsigned kvv = w * 1024 + lane * 16;
#define ISSUE(t) do { if ((t) < 32) S[t] = ld16<KAUX>(rk, kvv, (t) * 8192); else if ((t) < 64) S[t] = ld16<KAUX>(rv, kvv, ((t) - 32) * 8192); } while (0)
#pragma unroll
  for (int t = 0; t < R; ++t) { ISSUE(t); PIN(); }
  PIN();
  {
    float a[8] = {0.f, 0.f, 0.f, 0.f, 0.f, 0.f, 0.f, 0.f};
#pragma unroll
    for (int i = 0; i < 10; ++i) pair_fma(wb[2 * i], wb[2 * i + 1], xq[i >> 1][i & 1], a);
    float gp = 0.f;
#pragma unroll
    for (int i = 0; i < 10; ++i) gp = dot2(xq[i >> 1][i & 1], gq[i >> 1][i & 1], gp);
    KEEP8(a); KEEP(gp);
    PIN();
    {
      const unsigned voff = (unsigned)(80 * w + 10 * r8) * (FF * 2) + fs * 128 + c8 * 16;
#pragma unroll
      for (int i = 0; i < 10; ++i) { wb[i] = ld16<WAUX>(rw1, voff, i * (FF * 2)); wb[10 + i] = ld16<WAUX>(rw3, voff, i * (FF * 2)); }
    }
    PIN();
    *reinterpret_cast<f32x2*>(&s_q[w][8 * c + 2 * r]) = xrow_red8(a);
    s_g[w] = xrow_sum(gp);
  }
  STV(1);
  lds_barrier();
  KEEP(ep);
  const unsigned long long T = (((unsigned long long)(unsigned)__builtin_amdgcn_readfirstlane(ep[1]) << 32) | (unsigned)__builtin_amdgcn_readfirstlane(ep[0])) + 1ull;
  {
    const u32x2 tv = {(unsigned)T, (unsigned)(T >> 32)};
    __builtin_amdgcn_raw_buffer_store_b64(tv, rwsc, lane == 0 ? h * 8 : WS_BYTES, WS_READY + b * 256, AUX_SC1);
  }
  unsigned qp[4];
  float g;
  {
    f32x4 q0 = *reinterpret_cast<const f32x4*>(&s_q[2 * r][8 * c]) + *reinterpret_cast<const f32x4*>(&s_q[2 * r + 1][8 * c]);
    f32x4 q1 = *reinterpret_cast<const f32x4*>(&s_q[2 * r][8 * c + 4]) + *reinterpret_cast<const f32x4*>(&s_q[2 * r + 1][8 * c + 4]);
#pragma unroll
    for (int k = 0; k < 4; ++k) { q0[k] = xrow_sum(q0[k]); q1[k] = xrow_sum(q1[k]); }
    qp[0] = cvtpk(q0[0], q0[1]); qp[1] = cvtpk(q0[2], q0[3]); qp[2] = cvtpk(q1[0], q1[1]); qp[3] = cvtpk(q1[2], q1[3]);
    const f32x4 g0 = *reinterpret_cast<const f32x4*>(&s_g[0]), g1 = *reinterpret_cast<const f32x4*>(&s_g[4]);
    const float z = ((g0[0] + g0[1]) + (g0[2] + g0[3])) + ((g1[0] + g1[1]) + (g1[2] + g1[3]));
    g = 1.f / (1.f + __expf(-z));
  }
  PIN();
  constexpr float C2 = 0.08838834764831845f * 1.4426950408889634f;
  float sc[32];
  u32x4 w2b[10];
#pragma unroll
  for (int t = 0; t < 32; ++t) {
    float d = dot2(S[t][0], qp[0], 0.f);
    d = dot2(S[t][1], qp[1], d); d = dot2(S[t][2], qp[2], d); d = dot2(S[t][3], qp[3], d);
    KEEP(d);
    PIN();
    ISSUE(t + R);
    PIN();
    d += dpp<XOR1>(d); d += dpp<XOR2>(d); d += dpp<XOR7>(d); d += dpp<ROR8>(d);
    sc[t] = d * C2;
    KEEP(sc[t]);
    if (t == (R < 24 ? R : 24)) {
      float a[8] = {0.f, 0.f, 0.f, 0.f, 0.f, 0.f, 0.f, 0.f}, u[8] = {0.f, 0.f, 0.f, 0.f, 0.f, 0.f, 0.f, 0.f};
#pragma unroll
      for (int i = 0; i < 5; ++i) { pair_fma(wb[2 * i], wb[2 * i + 1], xf[i], a); pair_fma(wb[10 + 2 * i], wb[11 + 2 * i], xf[i], u); }
      KEEP8(a); KEEP8(u);
      PIN();
#pragma unroll
      for (int i = 0; i < 8; ++i) w2b[i] = ld16<WAUX>(rw2, lane * 16, (8 * w + i) * (DM * 2));
#pragma unroll
      for (int i = 0; i < 2; ++i) w2b[8 + i] = ld16<WAUX>(rw2, (2 * r + i) * (DM * 2) + 1024 + c * 16, 8 * w * (DM * 2));
      PIN();
      const f32x2 za = xrow_red8(a), zu = xrow_red8(u);
      const bool hb = lane & 8;
      s_gu[w][8 * c8 + r8] = (hb ? za[1] : za[0]) + dpp<ROR8>(hb ? za[0] : za[1]);
      s_gu[w][64 + 8 * c8 + r8] = (hb ? zu[1] : zu[0]) + dpp<ROR8>(hb ? zu[0] : zu[1]);
      PIN();
    }
  }
  float mw = sc[0];
#pragma unroll
  for (int t = 1; t < 32; ++t) mw = fmaxf(mw, sc[t]);
  mw = xrow_max(mw);
  float lw = 0.f;
#pragma unroll
  for (int t = 0; t < 32; ++t) { sc[t] = __builtin_amdgcn_exp2f(sc[t] - mw); lw += sc[t]; }
  lw = xrow_sum(lw);
  s_ml[w] = mw; s_ml[8 + w] = lw;
  lds_barrier();
  STV(2);
  unsigned pp[16];
  {
    const f32x4 m0 = *reinterpret_cast<const f32x4*>(&s_ml[0]), m1 = *reinterpret_cast<const f32x4*>(&s_ml[4]);
    const f32x4 l0 = *reinterpret_cast<const f32x4*>(&s_ml[8]), l1 = *reinterpret_cast<const f32x4*>(&s_ml[12]);
    const float M = fmaxf(fmaxf(fmaxf(m0[0], m0[1]), fmaxf(m0[2], m0[3])), fmaxf(fmaxf(m1[0], m1[1]), fmaxf(m1[2], m1[3])));
    float L = 0.f;
#pragma unroll
    for (int k = 0; k < 4; ++k) L += l0[k] * __builtin_amdgcn_exp2f(m0[k] - M) + l1[k] * __builtin_amdgcn_exp2f(m1[k] - M);
    const float f = __builtin_amdgcn_exp2f(mw - M) / L;
#pragma unroll
    for (int t = 0; t < 16; ++t) { pp[t] = cvtpk(sc[2 * t] * f, sc[2 * t + 1] * f); KEEP(pp[t]); }
  }
  float om[8] = {0.f, 0.f, 0.f, 0.f, 0.f, 0.f, 0.f, 0.f}, orr[8] = {0.f, 0.f, 0.f, 0.f, 0.f, 0.f, 0.f, 0.f};
  {
    float ga = s_gu[r8][8 * w + c8], up = s_gu[r8][64 + 8 * w + c8];
    ga += dpp<ROR8>(ga); ga = xrow_sum(ga); up += dpp<ROR8>(up); up = xrow_sum(up);
    const float hid = ga / (1.f + __expf(-ga)) * up;
    const unsigned hp = cvtpk(hid, dpp<XOR1>(hid));
    unsigned hs[4];
#pragma unroll
    for (int u = 0; u < 4; ++u) hs[u] = __builtin_amdgcn_readlane(hp, 2 * u);
#pragma unroll
    for (int u = 0; u < 4; ++u) pair_fma(w2b[2 * u], w2b[2 * u + 1], hs[u], om);
    pair_fma(w2b[8], w2b[9], row_pick<0>(hp), orr);
#pragma unroll
    for (int k = 0; k < 8; ++k) { om[k] *= 1.f - g; orr[k] *= 1.f - g; }
    KEEP8(om); KEEP8(orr);
  }
  PIN();
#pragma unroll
  for (int i = 0; i < 16; ++i) wb[i] = ld16<WAUX>(rwo, lane * 16, (16 * w + i) * (DM * 2));
#pragma unroll
  for (int i = 0; i < 4; ++i) wb[16 + i] = ld16<WAUX>(rwo, (2 * r + (i & 1) + 8 * (i >> 1)) * (DM * 2) + 1024 + c * 16, 16 * w * (DM * 2));
  const unsigned rdoff = (lane < NCONS ? lane : 0) * 8;
  u32x2 rdy = ld8<AUX_SC1>(rws0, rdoff, WS_READY + b * 256);
  PIN();
  float acc[8] = {0.f, 0.f, 0.f, 0.f, 0.f, 0.f, 0.f, 0.f};
  unsigned wp[80];
#pragma unroll
  for (int t = 0; t < 16; ++t) {
    pair_fma(S[32 + 2 * t], S[33 + 2 * t], pp[t], acc);
    KEEP8(acc);
    PIN();
    ISSUE(32 + 2 * t + R); ISSUE(33 + 2 * t + R);
    PIN();
    if (t == 13) {
#pragma unroll
      for (int u = 0; u < 10; ++u)
#pragma unroll
        for (int d = 0; d < 4; ++d) {
          wp[8 * u + 2 * d] = plo(wb[2 * u][d], wb[2 * u + 1][d]); wp[8 * u + 2 * d + 1] = phi(wb[2 * u][d], wb[2 * u + 1][d]);
          KEEP(wp[8 * u + 2 * d]); KEEP(wp[8 * u + 2 * d + 1]);
        }
      PIN();
    }
  }
  *reinterpret_cast<f32x2*>(&s_q[w][8 * c + 2 * r]) = xrow_red8(acc);
  STV(3);
  lds_barrier();
  STV(4);
  {
    const float o = xrow_sum(s_q[2 * r][16 * w + c] + s_q[2 * r + 1][16 * w + c]);
    const unsigned op = cvtpk(o, dpp<XOR1>(o));
    unsigned os[8];
#pragma unroll
    for (int u = 0; u < 8; ++u) os[u] = __builtin_amdgcn_readlane(op, 2 * u);
    float am[8] = {0.f, 0.f, 0.f, 0.f, 0.f, 0.f, 0.f, 0.f}, ar[8] = {0.f, 0.f, 0.f, 0.f, 0.f, 0.f, 0.f, 0.f};
    const unsigned c0 = row_pick<0>(op), c1 = row_pick<8>(op);
#pragma unroll
    for (int k = 0; k < 8; ++k) { ar[k] = dot2(wp[64 + k], c0, ar[k]); ar[k] = dot2(wp[72 + k], c1, ar[k]); }
#pragma unroll
    for (int u = 0; u < 8; ++u)
#pragma unroll
      for (int k = 0; k < 8; ++k) am[k] = dot2(wp[8 * u + k], os[u], am[k]);
#pragma unroll
    for (int k = 0; k < 8; ++k) { am[k] = fmaf(g, am[k], om[k]); ar[k] = fmaf(g, ar[k], orr[k]); }
    *reinterpret_cast<f32x4*>(&s_red[w][8 * lane]) = f32x4{am[0], am[1], am[2], am[3]};
    *reinterpret_cast<f32x4*>(&s_red[w][8 * lane + 4]) = f32x4{am[4], am[5], am[6], am[7]};
    *reinterpret_cast<f32x2*>(&s_red[w][512 + 8 * c + 2 * r]) = xrow_red8(ar);
  }
  if (w == 0) {
#pragma nounroll
    for (unsigned spins = 0; spins < SPIN_CAP; ++spins) {
      if (__all((((unsigned long long)rdy[1] << 32) | rdy[0]) == T)) break;
      __builtin_amdgcn_s_sleep(4);
      rdy = ld8<AUX_SC1>(rws, rdoff, WS_READY + b * 256);
    }
  }
  lds_barrier();
  STV(5);
  if (tid < DM / 4) {
    f32x4 s = *reinterpret_cast<const f32x4*>(&s_red[0][4 * tid]);
#pragma unroll
    for (int k = 1; k < 8; ++k) s += *reinterpret_cast<const f32x4*>(&s_red[k][4 * tid]);
    u32x4 sv = __builtin_bit_cast(u32x4, s);
#pragma unroll
    for (int k = 0; k < 4; ++k) sv[k] = sv[k] == SENT ? 0x7FC00000u : sv[k];
    __builtin_amdgcn_raw_buffer_store_b128(sv, rws, (tid >> 3) * 4096 + (tid & 7) * 16, WS_MAIL + b * NCONS * 4096 + h * 128, AUX_SC1);
  }
  if (w == 0 && PADK < 6) { const unsigned long long tg = stv[0] + 5000 + (stv[PADK < 6 ? PADK : 0] - stv[0]); while (__builtin_amdgcn_s_memrealtime() < tg) __builtin_amdgcn_s_sleep(1); }
  if (false) for (int k = 0; k < 6; ++k) *(unsigned long long*)(p.ws + (64 << 20) + (blockIdx.x * 16 + k) * 8) = stv[k];
  if (w == 3 && is_cons) {
    u32x4 m[4];
#pragma nounroll
    for (unsigned spins = 0; spins < SPIN_CAP; ++spins) {
#pragma unroll
      for (int i = 0; i < 4; ++i) m[i] = ld16<AUX_SC1>(rws, lane * 16 + i * 1024, WS_MAIL + (b * NCONS + h) * 4096);
      bool ok = true;
#pragma unroll
      for (int i = 0; i < 4; ++i)
#pragma unroll
        for (int k = 0; k < 4; ++k) ok = ok && (m[i][k] != SENT);
      if (__all(ok)) break;
      __builtin_amdgcn_s_sleep(2);
    }
    f32x4 s = (__builtin_bit_cast(f32x4, m[0]) + __builtin_bit_cast(f32x4, m[1])) + (__builtin_bit_cast(f32x4, m[2]) + __builtin_bit_cast(f32x4, m[3]));
#pragma unroll
    for (int k = 0; k < 4; ++k) { s[k] += dpp<ROR8>(s[k]); s[k] = xrow_sum(s[k]); }
    if (lane < 8) {
      const u32x2 ov = {cvtpk(s[0], s[1]), cvtpk(s[2], s[3])};
      *reinterpret_cast<u32x2*>(p.out + b * DM + h * 32 + lane * 4) = ov;
    }
    if (PADK == 6) { const unsigned long long tg = __builtin_amdgcn_s_memrealtime() + 5000; while (__builtin_amdgcn_s_memrealtime() < tg) __builtin_amdgcn_s_sleep(1); }
    if (h == 0) {
      const u32x2 e = ld8<AUX_SC1>(rws, 0, WS_READY + b * 256);
      if (lane == 0) __builtin_amdgcn_raw_buffer_store_b64(e, rws, 0, WS_EPOCH + b * 256, AUX_SC1);
    }
  }
}

extern "C" void kernel_launch(void* const* d_in, const int* in_sizes, int n_in, void* d_out, int out_size, void* d_ws,
                              size_t ws_size, hipStream_t stream) {
  Params p;
  p.x = (const uint16_t*)d_in[0]; p.k = (const uint16_t*)d_in[1]; p.v = (const uint16_t*)d_in[2];
  p.wq = (const uint16_t*)d_in[3]; p.wo = (const uint16_t*)d_in[4];
  p.w1 = (const uint16_t*)d_in[6]; p.w3 = (const uint16_t*)d_in[7]; p.w2 = (const uint16_t*)d_in[8];
  p.gw = (const uint16_t*)d_in[9];
  p.out = (uint16_t*)d_out; p.ws = (unsigned char*)d_ws;
  hipLaunchKernelGGL(gated_mix_decode_kernel, dim3(NB * NH), dim3(512), 0, stream, p);
}
```

```cpp
#include <hip/hip_runtime.h>
#include <hip/hip_bf16.h>
#include <stdint.h>

typedef __attribute__((ext_vector_type(2))) __bf16 bf16x2;
typedef __attribute__((ext_vector_type(4))) unsigned u32x4;
typedef __attribute__((ext_vector_type(2))) unsigned u32x2;
typedef __attribute__((ext_vector_type(4))) float f32x4;
typedef __attribute__((ext_vector_type(2))) float f32x2;
typedef __amdgpu_buffer_rsrc_t rsrc_t;

constexpr int NB = 8, DM = 640, NH = 32, SK = 4096, DH = 128, WIN = 1024, FF = 1728;
constexpr int NFS = FF / 64;
constexpr int NCONS = DM / 32;
#define RING 16
#define WAUX 2
#define KAUX 2
#define NOW 0
#define DSTEP 6
#define DEXTRA 2
constexpr int DS = DSTEP, DX = DEXTRA;
#define LAY 0
#define BSTEP 8
constexpr int BS = BSTEP;
constexpr int JGU = 35, JB2 = 37;
constexpr int wcnt(int j) { return j < 20 ? j + 1 : (20 + 5 * (j - 19) / 4 < 70 ? 20 + 5 * (j - 19) / 4 : 70); }
constexpr int wuse(int j) { return (j <= JB2 && wcnt(j) > 40 ? 40 : wcnt(j)) / 2; }
constexpr int kdone(int j) { return 3 * (j - 19) < j + 1 ? 3 * (j - 19) : (j + 1 < 32 ? j + 1 : 32); }
constexpr int AUX_NT = 2, AUX_SC1 = 16;
constexpr unsigned SENT = 0xFFFFFFFFu;
constexpr unsigned SPIN_CAP = 1u << 20;
constexpr unsigned WS_EPOCH = 0;
constexpr unsigned WS_READY = 4096;
constexpr unsigned WS_MAIL = 8192;
constexpr unsigned WS_BYTES = WS_MAIL + NB * NCONS * NH * 128;

__device__ __forceinline__ rsrc_t mkrsrc(const void* p, unsigned bytes) {
  return __builtin_amdgcn_make_buffer_rsrc(const_cast<void*>(p), 0, bytes, 0x00020000);
}
template <int AUX> __device__ __forceinline__ u32x4 ld16(rsrc_t r, unsigned voff, unsigned soff) {
  return __builtin_amdgcn_raw_buffer_load_b128(r, voff, soff, AUX);
}
template <int AUX> __device__ __forceinline__ u32x2 ld8(rsrc_t r, unsigned voff, unsigned soff) {
  return __builtin_amdgcn_raw_buffer_load_b64(r, voff, soff, AUX);
}
template <int AUX> __device__ __forceinline__ unsigned ld4(rsrc_t r, unsigned voff, unsigned soff) {
  return __builtin_amdgcn_raw_buffer_load_b32(r, voff, soff, AUX);
}
__device__ __forceinline__ float dot2(unsigned a, unsigned b, float acc) {
  return __builtin_amdgcn_fdot2_f32_bf16(__builtin_bit_cast(bf16x2, a), __builtin_bit_cast(bf16x2, b), acc, false);
}
__device__ __forceinline__ unsigned cvtpk(float lo, float hi) {
  bf16x2 v = {(__bf16)lo, (__bf16)hi};
  return __builtin_bit_cast(unsigned, v);
}
__device__ __forceinline__ unsigned plo(unsigned a, unsigned b) { return __builtin_amdgcn_perm(b, a, 0x05040100u); }
__device__ __forceinline__ unsigned phi(unsigned a, unsigned b) { return __builtin_amdgcn_perm(b, a, 0x07060302u); }
template <int CTRL> __device__ __forceinline__ float dpp(float x) {
  return __builtin_bit_cast(float, __builtin_amdgcn_mov_dpp(__builtin_bit_cast(int, x), CTRL, 0xf, 0xf, true));
}
constexpr int XOR1 = 0xB1, XOR2 = 0x4E, XOR7 = 0x141, ROR8 = 0x128;
__device__ __forceinline__ float xrow_sum(float x) {
  auto s = __builtin_amdgcn_permlane16_swap(__float_as_uint(x), __float_as_uint(x), false, false);
  x = __uint_as_float(s[0]) + __uint_as_float(s[1]);
  auto t = __builtin_amdgcn_permlane32_swap(__float_as_uint(x), __float_as_uint(x), false, false);
  return __uint_as_float(t[0]) + __uint_as_float(t[1]);
}
__device__ __forceinline__ float xrow_max(float x) {
  auto s = __builtin_amdgcn_permlane16_swap(__float_as_uint(x), __float_as_uint(x), false, false);
  x = fmaxf(__uint_as_float(s[0]), __uint_as_float(s[1]));
  auto t = __builtin_amdgcn_permlane32_swap(__float_as_uint(x), __float_as_uint(x), false, false);
  return fmaxf(__uint_as_float(t[0]), __uint_as_float(t[1]));
}
__device__ __forceinline__ float xrow_red4(float a, float b, float c, float d) {
  auto s = __builtin_amdgcn_permlane16_swap(__float_as_uint(a), __float_as_uint(b), false, false);
  auto t = __builtin_amdgcn_permlane16_swap(__float_as_uint(c), __float_as_uint(d), false, false);
  const float x = __uint_as_float(s[0]) + __uint_as_float(s[1]), y = __uint_as_float(t[0]) + __uint_as_float(t[1]);
  auto u = __builtin_amdgcn_permlane32_swap(__float_as_uint(x), __float_as_uint(y), false, false);
  return __uint_as_float(u[0]) + __uint_as_float(u[1]);
}
__device__ __forceinline__ f32x2 xrow_red8(const float* a) {
  return f32x2{xrow_red4(a[0], a[2], a[4], a[6]), xrow_red4(a[1], a[3], a[5], a[7])};
}
template <int L0> __device__ __forceinline__ unsigned row_pick(unsigned x) {
  int y = __builtin_amdgcn_mov_dpp((int)x, 0x150 + L0, 0xf, 0xf, true);
  y = __builtin_amdgcn_update_dpp(y, (int)x, 0x150 + L0 + 2, 0x2, 0xf, false);
  y = __builtin_amdgcn_update_dpp(y, (int)x, 0x150 + L0 + 4, 0x4, 0xf, false);
  y = __builtin_amdgcn_update_dpp(y, (int)x, 0x150 + L0 + 6, 0x8, 0xf, false);
  return (unsigned)y;
}
#define KEEP(x) asm volatile("" : "+v"(x))
#define KEEP8(a) asm volatile("" : "+v"(a[0]), "+v"(a[1]), "+v"(a[2]), "+v"(a[3]), "+v"(a[4]), "+v"(a[5]), "+v"(a[6]), "+v"(a[7]))
__device__ __forceinline__ void pair_fma(const u32x4& A, const u32x4& Bv, unsigned c, float* acc) {
#pragma unroll
  for (int d = 0; d < 4; ++d) {
    acc[2 * d] = dot2(plo(A[d], Bv[d]), c, acc[2 * d]);
    acc[2 * d + 1] = dot2(phi(A[d], Bv[d]), c, acc[2 * d + 1]);
  }
}
__device__ __forceinline__ void lds_barrier() {
  asm volatile("s_waitcnt lgkmcnt(0)\n\ts_barrier" ::: "memory");
}
template <int I> struct IC { static constexpr int v = I; };
template <int A, int B, class F> __device__ __forceinline__ void sfor(F&& f) {
  if constexpr (A < B) { f(IC<A>{}); sfor<A + 1, B>(f); }
}
#define LAM(x) [&](auto x) __attribute__((always_inline))
#define PIN() __builtin_amdgcn_sched_barrier(0)
#define STNOP() do { PIN(); asm volatile("s_nop 3"); PIN(); } while (0)
#define ST(k, cond)
#define STV(k)

struct Params {
  const uint16_t* x; const uint16_t* k; const uint16_t* v; const uint16_t* wq; const uint16_t* wo;
  const uint16_t* w1; const uint16_t* w3; const uint16_t* w2; const uint16_t* gw;
  uint16_t* out; unsigned char* ws;
};

__global__ void __launch_bounds__(512, 2) gated_mix_decode_kernel(Params p) {
  __shared__ __attribute__((aligned(16))) float s_q[8][DH];
  __shared__ __attribute__((aligned(16))) float s_gu[8][128];
  __shared__ __attribute__((aligned(16))) float s_red[8][DM];
  __shared__ __attribute__((aligned(16))) float s_g[8];
  __shared__ __attribute__((aligned(16))) float s_ml[16];

  const int tid = threadIdx.x, lane = tid & 63, r = lane >> 4, c = lane & 15;
  const int w = __builtin_amdgcn_readfirstlane(tid >> 6);
  const int bid = blockIdx.x;
#define MAP 2
  const int cj = bid & 31, b = bid >> 5;
  const int h = (MAP & 1) ? 4 * (bid & 7) + ((bid >> 3) & 3) : cj;
  const int f0 = (MAP & 2) ? ((NFS * (bid & 7)) >> 3) + ((bid >> 3) & 3) : cj;
  const bool has_ffn = (NOW & 4) ? false : f0 < ((MAP & 2) ? ((NFS * ((bid & 7) + 1)) >> 3) : NFS), is_cons = cj < NCONS;

  const rsrc_t rws = mkrsrc(p.ws, WS_BYTES);
  const rsrc_t rx = mkrsrc(p.x + b * DM, DM * 2), rgw = mkrsrc(p.gw, DM * 2);
  const size_t kvoff = ((size_t)(b * NH + h) * SK + (SK - WIN)) * DH;
  const rsrc_t rk = mkrsrc(p.k + kvoff, WIN * DH * 2), rv = mkrsrc(p.v + kvoff, WIN * DH * 2);
  const rsrc_t rwq = mkrsrc(p.wq, (NOW & 1) ? 0 : DM * NH * DH * 2), rwo = mkrsrc(p.wo + (size_t)h * DH * DM, (NOW & 2) ? 0 : DH * DM * 2);
  const int fs = has_ffn ? f0 : 0;
  const rsrc_t rw1 = mkrsrc(p.w1, has_ffn ? DM * FF * 2 : 0), rw3 = mkrsrc(p.w3, has_ffn ? DM * FF * 2 : 0);
  const rsrc_t rw2 = mkrsrc(p.w2 + (size_t)fs * 64 * DM, has_ffn ? 64 * DM * 2 : 0);
  const rsrc_t rws0 = mkrsrc(p.ws, w == 0 ? WS_BYTES : 0), rwsc = mkrsrc(p.ws, (w == 0 && is_cons) ? WS_BYTES : 0);

  STV(0);
  u32x2 ep = ld8<AUX_SC1>(rws0, 0, WS_EPOCH + b * 256);
  {
    const u32x4 sv = {SENT, SENT, SENT, SENT};
#pragma unroll
    for (int i = 0; i < 4; ++i)
      __builtin_amdgcn_raw_buffer_store_b128(sv, rwsc, lane * 16 + i * 1024, WS_MAIL + (b * NCONS + cj) * 4096, AUX_SC1);
  }
  STNOP();
  u32x2 xq[5], gq[5];
  unsigned xf[5];
  const int r8 = lane >> 3, c8 = lane & 7;
  const unsigned xoff = (LAY & 2) ? (8 * w + 2 * r) * 2 : (80 * w + 20 * r) * 2;
#pragma unroll
  for (int i = 0; i < 5; ++i) xq[i] = (LAY & 2) ? u32x2{ld4<0>(rx, xoff, 256 * i), ld4<0>(rx, xoff, 256 * i + 128)} : ld8<0>(rx, xoff + 8 * i, 0);
  PIN();
  u32x4 S[64], Wt[70];
  const unsigned kvv = w * 1024 + lane * 16;
  const unsigned vq = xoff * (NH * DH) + h * 256 + c * 16;
  const unsigned xfo = (LAY & 1) ? (16 * w + 2 * r8) * 2 : (80 * w + 10 * r8) * 2;
  const unsigned vf = xfo * FF + fs * 128 + c8 * 16;
  const unsigned vm = lane * 16, vr = 2 * r * (DM * 2) + 1024 + c * 16;
  const unsigned s2 = 8 * w * (DM * 2), so = 16 * w * (DM * 2);
  auto wload = [&](int i) __attribute__((always_inline)) -> u32x4 {
    if (i < 20) return ld16<WAUX>(rwq, vq, ((LAY & 2) ? 64 * (i >> 1) + (i & 1) : i) * (NH * DH * 2));
    if (i < 40) return ld16<WAUX>(((i >> 1) & 1) ? rw3 : rw1, vf, (((LAY & 1) ? 128 : 2) * ((i - 20) >> 2) + (i & 1)) * (FF * 2));
    if (i < 48) return ld16<WAUX>(rw2, vm + (i - 40) * (DM * 2), s2);
    if (i < 50) return ld16<WAUX>(rw2, vr + (i - 48) * (DM * 2), s2);
    if (i < 54) return ld16<WAUX>(rwo, vr + ((i & 1) + 8 * ((i - 50) >> 1)) * (DM * 2), so);
    return ld16<WAUX>(rwo, vm + (i - 54) * (DM * 2), so);
  };
  auto step = LAM(J) {
    constexpr int j = decltype(J)::v;
    if constexpr (j < 64) {
      sfor<wcnt(j - 1), wcnt(j)>(LAM(I) { Wt[decltype(I)::v] = wload(decltype(I)::v); PIN(); });
      S[j] = j < 32 ? ld16<KAUX>(rk, kvv, j * 8192) : ld16<KAUX>(rv, kvv, (j - 32) * 8192);
      PIN();
      if constexpr (BS > 0 && (j % (BS > 0 ? BS : 1)) == BS - 1 && j < 63) { asm volatile("s_barrier"); PIN(); }
    }
  };
  sfor<0, DS>(step);
#pragma unroll
  for (int i = 0; i < 5; ++i) { gq[i] = (LAY & 2) ? u32x2{ld4<0>(rgw, xoff, 256 * i), ld4<0>(rgw, xoff, 256 * i + 128)} : ld8<0>(rgw, xoff + 8 * i, 0); xf[i] = ld4<0>(rx, xfo + ((LAY & 1) ? 256 : 4) * i, 0); }
  PIN();
  {
    float a[8] = {0.f, 0.f, 0.f, 0.f, 0.f, 0.f, 0.f, 0.f};
    sfor<0, 10>(LAM(I) {
      constexpr int i = decltype(I)::v;
      pair_fma(Wt[2 * i], Wt[2 * i + 1], xq[i >> 1][i & 1], a);
      KEEP8(a);
      PIN();
      sfor<2 * i + DS, 2 * i + 2 + DS>(step);
    });
    sfor<20 + DS, 20 + DS + DX>(step);
    float gp = 0.f;
#pragma unroll
    for (int i = 0; i < 10; ++i) gp = dot2(xq[i >> 1][i & 1], gq[i >> 1][i & 1], gp);
    *reinterpret_cast<f32x2*>(&s_q[w][8 * c + 2 * r]) = xrow_red8(a);
    s_g[w] = xrow_sum(gp);
  }
  STV(1);
  lds_barrier();
  KEEP(ep);
  const unsigned long long T = (((unsigned long long)(unsigned)__builtin_amdgcn_readfirstlane(ep[1]) << 32) | (unsigned)__builtin_amdgcn_readfirstlane(ep[0])) + 1ull;
  {
    const u32x2 tv = {(unsigned)T, (unsigned)(T >> 32)};
    __builtin_amdgcn_raw_buffer_store_b64(tv, rwsc, lane == 0 ? cj * 8 : WS_BYTES, WS_READY + b * 256, AUX_SC1);
  }
  unsigned qp[4];
  float g;
  {
    f32x4 q0 = *reinterpret_cast<const f32x4*>(&s_q[2 * r][8 * c]) + *reinterpret_cast<const f32x4*>(&s_q[2 * r + 1][8 * c]);
    f32x4 q1 = *reinterpret_cast<const f32x4*>(&s_q[2 * r][8 * c + 4]) + *reinterpret_cast<const f32x4*>(&s_q[2 * r + 1][8 * c + 4]);
#pragma unroll
    for (int k = 0; k < 4; ++k) { q0[k] = xrow_sum(q0[k]); q1[k] = xrow_sum(q1[k]); }
    qp[0] = cvtpk(q0[0], q0[1]); qp[1] = cvtpk(q0[2], q0[3]); qp[2] = cvtpk(q1[0], q1[1]); qp[3] = cvtpk(q1[2], q1[3]);
    const f32x4 g0 = *reinterpret_cast<const f32x4*>(&s_g[0]), g1 = *reinterpret_cast<const f32x4*>(&s_g[4]);
    const float z = ((g0[0] + g0[1]) + (g0[2] + g0[3])) + ((g1[0] + g1[1]) + (g1[2] + g1[3]));
    g = 1.f / (1.f + __expf(-z));
    KEEP(g);
  }
  PIN();
  constexpr float C2 = 0.08838834764831845f * 1.4426950408889634f;
  float sc[32], mw, f;
  float ga8[8] = {0.f, 0.f, 0.f, 0.f, 0.f, 0.f, 0.f, 0.f}, up8[8] = {0.f, 0.f, 0.f, 0.f, 0.f, 0.f, 0.f, 0.f};
  float om[8] = {0.f, 0.f, 0.f, 0.f, 0.f, 0.f, 0.f, 0.f}, orr[8] = {0.f, 0.f, 0.f, 0.f, 0.f, 0.f, 0.f, 0.f};
  float acc[8] = {0.f, 0.f, 0.f, 0.f, 0.f, 0.f, 0.f, 0.f};
  unsigned pp[16], wp[80], hs[4], hr;
  const unsigned rdoff = (lane < NCONS ? lane : 0) * 8;
  u32x2 rdy;
  sfor<20, 64>(LAM(J) {
    constexpr int j = decltype(J)::v;
#pragma unroll
    for (int t = kdone(j - 1); t < kdone(j); ++t) {
      float d = dot2(S[t][0], qp[0], 0.f);
      d = dot2(S[t][1], qp[1], d); d = dot2(S[t][2], qp[2], d); d = dot2(S[t][3], qp[3], d);
      d += dpp<XOR1>(d); d += dpp<XOR2>(d); d += dpp<XOR7>(d); d += dpp<ROR8>(d);
      sc[t] = d * C2;
      KEEP(sc[t]);
    }
#pragma unroll
    for (int u = wuse(j - 1); u < wuse(j); ++u) {
      if (u < 20) { if (u & 1) { pair_fma(Wt[2 * u], Wt[2 * u + 1], xf[(u - 10) >> 1], up8); KEEP8(up8); }
                    else { pair_fma(Wt[2 * u], Wt[2 * u + 1], xf[(u - 10) >> 1], ga8); KEEP8(ga8); } }
      else if (u < 24) { pair_fma(Wt[2 * u], Wt[2 * u + 1], hs[u - 20], om); KEEP8(om); }
      else if (u < 25) { pair_fma(Wt[2 * u], Wt[2 * u + 1], hr, orr); KEEP8(orr); }
      else {
        const int o = u < 27 ? 64 + 8 * (u - 25) : 8 * (u - 27);
#pragma unroll
        for (int d = 0; d < 4; ++d) {
          wp[o + 2 * d] = plo(Wt[2 * u][d], Wt[2 * u + 1][d]); wp[o + 2 * d + 1] = phi(Wt[2 * u][d], Wt[2 * u + 1][d]);
          KEEP(wp[o + 2 * d]); KEEP(wp[o + 2 * d + 1]);
        }
      }
    }
    if constexpr (j > 32 && (j & 1)) { pair_fma(S[j - 1], S[j], pp[(j - 33) >> 1], acc); KEEP8(acc); }
    PIN();
    if constexpr (j >= 20 + DX) step(IC<j + DS>{});
    if constexpr (j == 48) { rdy = ld8<AUX_SC1>(rws0, rdoff, WS_READY + b * 256); PIN(); }
    if constexpr (j == 31) {
      mw = sc[0];
#pragma unroll
      for (int t = 1; t < 32; ++t) mw = fmaxf(mw, sc[t]);
      mw = xrow_max(mw);
      float lw = 0.f;
#pragma unroll
      for (int t = 0; t < 16; ++t) {
        const float e0 = __builtin_amdgcn_exp2f(sc[2 * t] - mw), e1 = __builtin_amdgcn_exp2f(sc[2 * t + 1] - mw);
        lw += e0 + e1; pp[t] = cvtpk(e0, e1); KEEP(pp[t]);
      }
      lw = xrow_sum(lw);
      s_ml[w] = mw; s_ml[8 + w] = lw;
      PIN();
    }
    if constexpr (j == JGU) {
      const f32x2 za = xrow_red8(ga8), zu = xrow_red8(up8);
      const bool hb = lane & 8;
      s_gu[w][8 * c8 + r8] = (hb ? za[1] : za[0]) + dpp<ROR8>(hb ? za[0] : za[1]);
      s_gu[w][64 + 8 * c8 + r8] = (hb ? zu[1] : zu[0]) + dpp<ROR8>(hb ? zu[0] : zu[1]);
      PIN();
    }
    if constexpr (j == JB2) {
      lds_barrier();
      STV(2);
      const f32x4 m0 = *reinterpret_cast<const f32x4*>(&s_ml[0]), m1 = *reinterpret_cast<const f32x4*>(&s_ml[4]);
      const f32x4 l0 = *reinterpret_cast<const f32x4*>(&s_ml[8]), l1 = *reinterpret_cast<const f32x4*>(&s_ml[12]);
      const float M = fmaxf(fmaxf(fmaxf(m0[0], m0[1]), fmaxf(m0[2], m0[3])), fmaxf(fmaxf(m1[0], m1[1]), fmaxf(m1[2], m1[3])));
      float L = 0.f;
#pragma unroll
      for (int k = 0; k < 4; ++k) L += l0[k] * __builtin_amdgcn_exp2f(m0[k] - M) + l1[k] * __builtin_amdgcn_exp2f(m1[k] - M);
      f = __builtin_amdgcn_exp2f(mw - M) / L;
      KEEP(f);
      float ga = s_gu[r8][8 * w + c8], up = s_gu[r8][64 + 8 * w + c8];
      ga += dpp<ROR8>(ga); ga = xrow_sum(ga); up += dpp<ROR8>(up); up = xrow_sum(up);
      const float hid = ga / (1.f + __expf(-ga)) * up;
      const unsigned hp = cvtpk(hid, dpp<XOR1>(hid));
#pragma unroll
      for (int u = 0; u < 4; ++u) hs[u] = __builtin_amdgcn_readlane(hp, 2 * u);
      hr = row_pick<0>(hp);
      KEEP(hr);
      PIN();
    }
    if constexpr (j == 50) {
#pragma unroll
      for (int k = 0; k < 8; ++k) { om[k] *= 1.f - g; orr[k] *= 1.f - g; }
      KEEP8(om); KEEP8(orr);
      PIN();
    }
  });
  *reinterpret_cast<f32x2*>(&s_q[w][8 * c + 2 * r]) = xrow_red8(acc) * f;
  STV(3);
  lds_barrier();
  STV(4);
  {
    const float o = xrow_sum(s_q[2 * r][16 * w + c] + s_q[2 * r + 1][16 * w + c]);
    const unsigned op = cvtpk(o, dpp<XOR1>(o));
    unsigned os[8];
#pragma unroll
    for (int u = 0; u < 8; ++u) os[u] = __builtin_amdgcn_readlane(op, 2 * u);
    float am[8] = {0.f, 0.f, 0.f, 0.f, 0.f, 0.f, 0.f, 0.f}, ar[8] = {0.f, 0.f, 0.f, 0.f, 0.f, 0.f, 0.f, 0.f};
    const unsigned c0 = row_pick<0>(op), c1 = row_pick<8>(op);
#pragma unroll
    for (int k = 0; k < 8; ++k) { ar[k] = dot2(wp[64 + k], c0, ar[k]); ar[k] = dot2(wp[72 + k], c1, ar[k]); }
#pragma unroll
    for (int u = 0; u < 8; ++u)
#pragma unroll
      for (int k = 0; k < 8; ++k) am[k] = dot2(wp[8 * u + k], os[u], am[k]);
#pragma unroll
    for (int k = 0; k < 8; ++k) { am[k] = fmaf(g, am[k], om[k]); ar[k] = fmaf(g, ar[k], orr[k]); }
    *reinterpret_cast<f32x4*>(&s_red[w][8 * lane]) = f32x4{am[0], am[1], am[2], am[3]};
    *reinterpret_cast<f32x4*>(&s_red[w][8 * lane + 4]) = f32x4{am[4], am[5], am[6], am[7]};
    *reinterpret_cast<f32x2*>(&s_red[w][512 + 8 * c + 2 * r]) = xrow_red8(ar);
  }
  if (w == 0) {
#pragma nounroll
    for (unsigned spins = 0; spins < SPIN_CAP; ++spins) {
      if (__all((((unsigned long long)rdy[1] << 32) | rdy[0]) == T)) break;
      __builtin_amdgcn_s_sleep(4);
      rdy = ld8<AUX_SC1>(rws, rdoff, WS_READY + b * 256);
    }
  }
  lds_barrier();
  STV(5);
  if (tid < DM / 4) {
    f32x4 s = *reinterpret_cast<const f32x4*>(&s_red[0][4 * tid]);
#pragma unroll
    for (int k = 1; k < 8; ++k) s += *reinterpret_cast<const f32x4*>(&s_red[k][4 * tid]);
    u32x4 sv = __builtin_bit_cast(u32x4, s);
#pragma unroll
    for (int k = 0; k < 4; ++k) sv[k] = sv[k] == SENT ? 0x7FC00000u : sv[k];
    __builtin_amdgcn_raw_buffer_store_b128(sv, rws, (tid >> 3) * 4096 + (tid & 7) * 16, WS_MAIL + b * NCONS * 4096 + h * 128, AUX_SC1);
    STNOP();
  }
  if (w == 3 && is_cons) {
    u32x4 m[4];
#pragma nounroll
    for (unsigned spins = 0; spins < SPIN_CAP; ++spins) {
#pragma unroll
      for (int i = 0; i < 4; ++i) m[i] = ld16<AUX_SC1>(rws, lane * 16 + i * 1024, WS_MAIL + (b * NCONS + cj) * 4096);
      bool ok = true;
#pragma unroll
      for (int i = 0; i < 4; ++i)
#pragma unroll
        for (int k = 0; k < 4; ++k) ok = ok && (m[i][k] != SENT);
      if (__all(ok)) break;
      __builtin_amdgcn_s_sleep(2);
    }
    f32x4 s = (__builtin_bit_cast(f32x4, m[0]) + __builtin_bit_cast(f32x4, m[1])) + (__builtin_bit_cast(f32x4, m[2]) + __builtin_bit_cast(f32x4, m[3]));
#pragma unroll
    for (int k = 0; k < 4; ++k) { s[k] += dpp<ROR8>(s[k]); s[k] = xrow_sum(s[k]); }
    if (lane < 8) {
      const u32x2 ov = {cvtpk(s[0], s[1]), cvtpk(s[2], s[3])};
      *reinterpret_cast<u32x2*>(p.out + b * DM + cj * 32 + lane * 4) = ov;
    }
    ST(6, true);
    if (cj == 0) {
      const u32x2 e = ld8<AUX_SC1>(rws, 0, WS_READY + b * 256);
      if (lane == 0) __builtin_amdgcn_raw_buffer_store_b64(e, rws, 0, WS_EPOCH + b * 256, AUX_SC1);
    }
  }
}

extern "C" void kernel_launch(void* const* d_in, const int* in_sizes, int n_in, void* d_out, int out_size, void* d_ws,
                              size_t ws_size, hipStream_t stream) {
  Params p;
  p.x = (const uint16_t*)d_in[0]; p.k = (const uint16_t*)d_in[1]; p.v = (const uint16_t*)d_in[2];
  p.wq = (const uint16_t*)d_in[3]; p.wo = (const uint16_t*)d_in[4];
  p.w1 = (const uint16_t*)d_in[6]; p.w3 = (const uint16_t*)d_in[7]; p.w2 = (const uint16_t*)d_in[8];
  p.gw = (const uint16_t*)d_in[9];
  p.out = (uint16_t*)d_out; p.ws = (unsigned char*)d_ws;
  hipLaunchKernelGGL(gated_mix_decode_kernel, dim3(NB * NH), dim3(512), 0, stream, p);
}
```

```cpp
#include <hip/hip_runtime.h>
#include <hip/hip_bf16.h>
#include <stdint.h>

typedef __attribute__((ext_vector_type(2))) __bf16 bf16x2;
typedef __attribute__((ext_vector_type(4))) unsigned u32x4;
typedef __attribute__((ext_vector_type(2))) unsigned u32x2;
typedef __attribute__((ext_vector_type(4))) float f32x4;
typedef __attribute__((ext_vector_type(2))) float f32x2;
typedef __amdgpu_buffer_rsrc_t rsrc_t;

constexpr int NB = 8, DM = 640, NH = 32, SK = 4096, DH = 128, WIN = 1024, FF = 1728;
constexpr int NFS = FF / 64;
constexpr int NCONS = DM / 32;
#define RING 16
#define WAUX 2
#define KAUX 2
#define NOW 0
#define DSTEP 6
#define DEXTRA 2
constexpr int DS = DSTEP, DX = DEXTRA;
#define LAY 0
#define BSTEP 8
constexpr int BS = BSTEP;
#define JGATH 15
#define JWO 22
constexpr int JG = JGATH, JB2 = 35;
#define DFRONT 5
#define FBAR 1
#define DG 0
#define XS 18
#define XL 18
#define MS 16
#define ML 16
#define NOSELF 1
#define MC 16
#define PSL 0
#define JW2 12
constexpr int D0 = DFRONT;
constexpr int wcnt(int j) { return j < 0 ? 0 : j < JW2 ? 10 : j < JW2 + 10 ? j - JW2 + 11 : j < JWO ? 20 : (21 + (j - JWO) / 2 < 40 ? 21 + (j - JWO) / 2 : 40); }
constexpr int wuse(int j) { return j <= JG ? 5 : (5 + (j - JG) < wcnt(j) / 2 ? 5 + (j - JG) : wcnt(j) / 2); }
constexpr int kdone(int j) { return 3 * (j - JG) < j + 1 && 3 * (j - JG) < 32 ? 3 * (j - JG) : (j + 1 < 32 ? j + 1 : 32); }
static_assert(wcnt(60) == 40 && kdone(31) == 32 && D0 <= JG && JW2 + 10 <= JWO, "schedule");
constexpr int AUX_NT = 2, AUX_SC1 = 16;
constexpr unsigned SENT = 0xFFFFFFFFu;
constexpr unsigned SPIN_CAP = 1u << 20;
constexpr unsigned WS_EPOCH = 0;
constexpr unsigned WS_READY = 4096;
constexpr unsigned WS_MAIL = 8192;
constexpr unsigned WS_X = WS_MAIL + NB * NCONS * NH * 128;
constexpr unsigned WS_BYTES = WS_X + NB * NH * 8192;

__device__ __forceinline__ rsrc_t mkrsrc(const void* p, unsigned bytes) {
  return __builtin_amdgcn_make_buffer_rsrc(const_cast<void*>(p), 0, bytes, 0x00020000);
}
template <int AUX> __device__ __forceinline__ u32x4 ld16(rsrc_t r, unsigned voff, unsigned soff) {
  return __builtin_amdgcn_raw_buffer_load_b128(r, voff, soff, AUX);
}
template <int AUX> __device__ __forceinline__ u32x2 ld8(rsrc_t r, unsigned voff, unsigned soff) {
  return __builtin_amdgcn_raw_buffer_load_b64(r, voff, soff, AUX);
}
template <int AUX> __device__ __forceinline__ unsigned ld4(rsrc_t r, unsigned voff, unsigned soff) {
  return __builtin_amdgcn_raw_buffer_load_b32(r, voff, soff, AUX);
}
__device__ __forceinline__ float dot2(unsigned a, unsigned b, float acc) {
  return __builtin_amdgcn_fdot2_f32_bf16(__builtin_bit_cast(bf16x2, a), __builtin_bit_cast(bf16x2, b), acc, false);
}
__device__ __forceinline__ unsigned cvtpk(float lo, float hi) {
  bf16x2 v = {(__bf16)lo, (__bf16)hi};
  return __builtin_bit_cast(unsigned, v);
}
__device__ __forceinline__ unsigned plo(unsigned a, unsigned b) { return __builtin_amdgcn_perm(b, a, 0x05040100u); }
__device__ __forceinline__ unsigned phi(unsigned a, unsigned b) { return __builtin_amdgcn_perm(b, a, 0x07060302u); }
template <int CTRL> __device__ __forceinline__ float dpp(float x) {
  return __builtin_bit_cast(float, __builtin_amdgcn_mov_dpp(__builtin_bit_cast(int, x), CTRL, 0xf, 0xf, true));
}
constexpr int XOR1 = 0xB1, XOR2 = 0x4E, XOR7 = 0x141, ROR8 = 0x128;
__device__ __forceinline__ float xrow_sum(float x) {
  auto s = __builtin_amdgcn_permlane16_swap(__float_as_uint(x), __float_as_uint(x), false, false);
  x = __uint_as_float(s[0]) + __uint_as_float(s[1]);
  auto t = __builtin_amdgcn_permlane32_swap(__float_as_uint(x), __float_as_uint(x), false, false);
  return __uint_as_float(t[0]) + __uint_as_float(t[1]);
}
__device__ __forceinline__ float xrow_max(float x) {
  auto s = __builtin_amdgcn_permlane16_swap(__float_as_uint(x), __float_as_uint(x), false, false);
  x = fmaxf(__uint_as_float(s[0]), __uint_as_float(s[1]));
  auto t = __builtin_amdgcn_permlane32_swap(__float_as_uint(x), __float_as_uint(x), false, false);
  return fmaxf(__uint_as_float(t[0]), __uint_as_float(t[1]));
}
__device__ __forceinline__ float xrow_red4(float a, float b, float c, float d) {
  auto s = __builtin_amdgcn_permlane16_swap(__float_as_uint(a), __float_as_uint(b), false, false);
  auto t = __builtin_amdgcn_permlane16_swap(__float_as_uint(c), __float_as_uint(d), false, false);
  const float x = __uint_as_float(s[0]) + __uint_as_float(s[1]), y = __uint_as_float(t[0]) + __uint_as_float(t[1]);
  auto u = __builtin_amdgcn_permlane32_swap(__float_as_uint(x), __float_as_uint(y), false, false);
  return __uint_as_float(u[0]) + __uint_as_float(u[1]);
}
__device__ __forceinline__ f32x2 xrow_red8(const float* a) {
  return f32x2{xrow_red4(a[0], a[2], a[4], a[6]), xrow_red4(a[1], a[3], a[5], a[7])};
}
template <int L0> __device__ __forceinline__ unsigned row_pick(unsigned x) {
  int y = __builtin_amdgcn_mov_dpp((int)x, 0x150 + L0, 0xf, 0xf, true);
  y = __builtin_amdgcn_update_dpp(y, (int)x, 0x150 + L0 + 2, 0x2, 0xf, false);
  y = __builtin_amdgcn_update_dpp(y, (int)x, 0x150 + L0 + 4, 0x4, 0xf, false);
  y = __builtin_amdgcn_update_dpp(y, (int)x, 0x150 + L0 + 6, 0x8, 0xf, false);
  return (unsigned)y;
}
#define KEEP(x) asm volatile("" : "+v"(x))
#define KEEP8(a) asm volatile("" : "+v"(a[0]), "+v"(a[1]), "+v"(a[2]), "+v"(a[3]), "+v"(a[4]), "+v"(a[5]), "+v"(a[6]), "+v"(a[7]))
__device__ __forceinline__ void pair_fma(const u32x4& A, const u32x4& Bv, unsigned c, float* acc) {
#pragma unroll
  for (int d = 0; d < 4; ++d) {
    acc[2 * d] = dot2(plo(A[d], Bv[d]), c, acc[2 * d]);
    acc[2 * d + 1] = dot2(phi(A[d], Bv[d]), c, acc[2 * d + 1]);
  }
}
typedef __attribute__((ext_vector_type(4))) short s16x4;
__device__ __forceinline__ f32x4 mma4(u32x2 a, u32x2 bv, f32x4 d) {
  return __builtin_amdgcn_mfma_f32_4x4x4bf16_1k(__builtin_bit_cast(s16x4, a), __builtin_bit_cast(s16x4, bv), d, 0, 0, 0);
}
__device__ __forceinline__ unsigned pcol(const u32x4& A, const u32x4& Bv, int k) { return (k & 1) ? phi(A[k >> 1], Bv[k >> 1]) : plo(A[k >> 1], Bv[k >> 1]); }
__device__ __forceinline__ void lds_barrier() {
  asm volatile("s_waitcnt lgkmcnt(0)\n\ts_barrier" ::: "memory");
}
template <int I> struct IC { static constexpr int v = I; };
template <int A, int B, class F> __device__ __forceinline__ void sfor(F&& f) {
  if constexpr (A < B) { f(IC<A>{}); sfor<A + 1, B>(f); }
}
#define LAM(x) [&](auto x) __attribute__((always_inline))
#define PIN() __builtin_amdgcn_sched_barrier(0)
#define STNOP() do { PIN(); asm volatile("s_nop 3"); PIN(); } while (0)
#define ST(k, cond)
#define STV(k)

struct Params {
  const uint16_t* x; const uint16_t* k; const uint16_t* v; const uint16_t* wq; const uint16_t* wo;
  const uint16_t* w1; const uint16_t* w3; const uint16_t* w2; const uint16_t* gw;
  uint16_t* out; unsigned char* ws;
};

__global__ void __launch_bounds__(512, 2) gated_mix_decode_kernel(Params p) {
  __shared__ __attribute__((aligned(16))) float s_q[8][DH];
  __shared__ __attribute__((aligned(16))) float s_x[9][128];
  __shared__ __attribute__((aligned(16))) float s_red[8][DM];
  __shared__ __attribute__((aligned(16))) float s_ml[16];
  __shared__ u32x2 s_ep[5];

  const int tid = threadIdx.x, lane = tid & 63, r = lane >> 4, c = lane & 15;
  const int w = __builtin_amdgcn_readfirstlane(tid >> 6);
  const int bid = blockIdx.x;
#define MAP 2
  const int cj = bid & 31, b = bid >> 5;
  const int h = (MAP & 1) ? 4 * (bid & 7) + ((bid >> 3) & 3) : cj;
  const int f0 = (MAP & 2) ? ((NFS * (bid & 7)) >> 3) + ((bid >> 3) & 3) : cj;
  const bool has_ffn = (NOW & 4) ? false : f0 < ((MAP & 2) ? ((NFS * ((bid & 7) + 1)) >> 3) : NFS), is_cons = cj < NCONS;

  const rsrc_t rws = mkrsrc(p.ws, WS_BYTES);
  const rsrc_t rx = mkrsrc(p.x, NB * DM * 2), rgw = mkrsrc(p.gw, DM * 2);
  const size_t kvoff = ((size_t)(b * NH + h) * SK + (SK - WIN)) * DH;
  const rsrc_t rk = mkrsrc(p.k + kvoff, WIN * DH * 2), rv = mkrsrc(p.v + kvoff, WIN * DH * 2);
  const rsrc_t rwo = mkrsrc(p.wo + (size_t)h * DH * DM, (NOW & 2) ? 0 : DH * DM * 2);
  const int fs = has_ffn ? f0 : 0;
  const rsrc_t rw2 = mkrsrc(p.w2 + (size_t)fs * 64 * DM, has_ffn ? 64 * DM * 2 : 0);
  const rsrc_t rws0 = mkrsrc(p.ws, w == 0 ? WS_BYTES : 0), rwsc = mkrsrc(p.ws, (w == 0 && is_cons) ? WS_BYTES : 0);

  STV(0);
  const int bg = b & 4, bi = b & 3, wr = w & 3, wm = w >> 2;
  u32x2 ep = ld8<AUX_SC1>(rws0, lane < 4 ? (bg + lane) * 256 : WS_BYTES, WS_EPOCH);
  {
    const u32x4 sv = {SENT, SENT, SENT, SENT};
#pragma unroll
    for (int i = 0; i < 4; ++i)
      __builtin_amdgcn_raw_buffer_store_b128(sv, rwsc, lane * 16 + i * 1024, WS_MAIL + (b * NCONS + cj) * 4096, MC);
  }
  STNOP();
  const int r8 = lane >> 3, c8 = lane & 7, rb = 160 * bi + 40 * wr + 10 * r;
  u32x4 ga4, go4;
  unsigned ga1, go1;
  const u32x4 xa = ld16<0>(rx, rb * 2 + (c & 3) * (DM * 2), bg * (DM * 2));
  const unsigned xb = ld4<0>(rx, rb * 2 + 16 + (c & 3) * (DM * 2), bg * (DM * 2));
  PIN();
  u32x4 S[64], Wt[40], pk;
  const unsigned kvv = w * 1024 + lane * 16;
  const unsigned wst = wm ? FF * 2 : NH * DH * 2;
  const unsigned char* wptr = (wm ? (const unsigned char*)(c < 8 ? p.w1 : p.w3) + fs * 128 + c8 * 16 : (const unsigned char*)p.wq + h * 256 + c * 16) + (size_t)rb * wst;
  const unsigned vm = lane * 16, vr = 2 * r * (DM * 2) + 1024 + c * 16;
  const unsigned s2 = 8 * w * (DM * 2), so = 16 * w * (DM * 2), sx = WS_X + (b * NH + cj) * 8192;
  auto wload = [&](int i) __attribute__((always_inline)) -> u32x4 {
    if (i < 10) return __builtin_nontemporal_load((const __attribute__((address_space(1))) u32x4*)(wptr + i * wst));
    if (i < 18) return ld16<WAUX>(rw2, vm + (i - 10) * (DM * 2), s2);
    if (i < 20) return ld16<WAUX>(rw2, vr + (i - 18) * (DM * 2), s2);
    if (i < 24) return ld16<WAUX>(rwo, vr + ((i & 1) + 8 * ((i - 20) >> 1)) * (DM * 2), so);
    return ld16<WAUX>(rwo, vm + (i - 24) * (DM * 2), so);
  };
  auto step = LAM(J) {
    constexpr int j = decltype(J)::v;
    if constexpr (j < 64) {
      sfor<wcnt(j - 1), wcnt(j)>(LAM(I) { Wt[decltype(I)::v] = wload(decltype(I)::v); PIN(); });
      if constexpr (j == 0 && FBAR) { asm volatile("s_barrier"); PIN(); }
      S[j] = j < 32 ? ld16<KAUX>(rk, kvv, j * 8192) : ld16<KAUX>(rv, kvv, (j - 32) * 8192);
      PIN();
      if constexpr (j == JG) { pk = ld16<XL>(mkrsrc(p.ws, (DG & 2) || (NOSELF && (w >> 1) == bi) ? 0 : WS_BYTES), kvv, sx); PIN(); }
      if constexpr (BS > 0 && (j % (BS > 0 ? BS : 1)) == BS - 1 && j < 63) { asm volatile("s_barrier"); PIN(); }
    }
  };
  sfor<0, D0>(step);
  {
    f32x4 a[8];
#pragma unroll
    for (int k = 0; k < 8; ++k) a[k] = mma4(u32x2{xa[0], xa[1]}, u32x2{pcol(Wt[0], Wt[1], k), pcol(Wt[2], Wt[3], k)}, f32x4{0.f, 0.f, 0.f, 0.f});
    KEEP8(a); PIN();
#pragma unroll
    for (int k = 0; k < 8; ++k) a[k] = mma4(u32x2{xa[2], xa[3]}, u32x2{pcol(Wt[4], Wt[5], k), pcol(Wt[6], Wt[7], k)}, a[k]);
    KEEP8(a); PIN();
#pragma unroll
    for (int k = 0; k < 8; ++k) a[k] = mma4(u32x2{xb, 0u}, u32x2{pcol(Wt[8], Wt[9], k), 0u}, a[k]);
    f32x4 z0, z1;
#pragma unroll
    for (int k = 0; k < 4; ++k) { z0[k] = xrow_red4(a[k][0], a[k][1], a[k][2], a[k][3]); z1[k] = xrow_red4(a[4 + k][0], a[4 + k][1], a[4 + k][2], a[4 + k][3]); }
    *reinterpret_cast<f32x4*>(&s_red[w][128 * r + 8 * c]) = z0;
    *reinterpret_cast<f32x4*>(&s_red[w][128 * r + 8 * c + 4]) = z1;
  }
  s_ep[w == 0 && lane < 4 ? lane : 4] = ep;
  lds_barrier();
  ep = s_ep[(lane & 1) ? wr : bi];
  KEEP(ep);
  const unsigned long long T = (((unsigned long long)(unsigned)__builtin_amdgcn_readlane(ep[1], 0) << 32) | (unsigned)__builtin_amdgcn_readlane(ep[0], 0)) + 1ull;
  {
    const unsigned long long Td = (((unsigned long long)(unsigned)__builtin_amdgcn_readlane(ep[1], 1) << 32) | (unsigned)__builtin_amdgcn_readlane(ep[0], 1)) + 1ull;
    f32x2 v = *reinterpret_cast<const f32x2*>(&s_red[4 * wm][128 * wr + 2 * lane]);
#pragma unroll
    for (int k = 1; k < 4; ++k) v += *reinterpret_cast<const f32x2*>(&s_red[4 * wm + k][128 * wr + 2 * lane]);
    const u32x4 o = {__float_as_uint(v[0]), __float_as_uint(v[1]), (unsigned)Td, (unsigned)(Td >> 32)};
    __builtin_amdgcn_raw_buffer_store_b128(o, mkrsrc(p.ws, (DG & 1) || (NOSELF && wr == bi) ? 0 : WS_BYTES), vm, WS_X + ((bg + wr) * NH + cj) * 8192 + (2 * bi + wm) * 1024, XS);
    STNOP();
    if (NOSELF) *reinterpret_cast<f32x2*>(&s_x[wr == bi ? 2 * bi + wm : 8][2 * lane]) = v;
    const u32x2 tv = {(unsigned)T, (unsigned)(T >> 32)};
    __builtin_amdgcn_raw_buffer_store_b64(tv, rwsc, lane == 0 ? cj * 8 : WS_BYTES, WS_READY + b * 256, AUX_SC1);
    PIN();
  }
  STV(6);
  ga4 = ld16<0>(rgw, lane * 20, 0); ga1 = ld4<0>(rgw, lane * 20 + 16, 0);
  go4 = ld16<0>(rx, lane * 20, b * (DM * 2)); go1 = ld4<0>(rx, lane * 20 + 16, b * (DM * 2));
  PIN();
  sfor<D0, JG + 1 + DS + DX>(step);
  float g;
  {
    float z = dot2(go1, ga1, 0.f);
#pragma unroll
    for (int k = 0; k < 4; ++k) z = dot2(go4[k], ga4[k], z);
    z += dpp<XOR1>(z); z += dpp<XOR2>(z); z += dpp<XOR7>(z); z += dpp<ROR8>(z);
    g = 1.f / (1.f + __expf(-xrow_sum(z)));
    KEEP(g);
    PIN();
  }
  {
    const unsigned long long bad = __builtin_amdgcn_ballot_w64(pk[2] != (unsigned)T || pk[3] != (unsigned)(T >> 32)) & (DG || (NOSELF && (w >> 1) == bi) ? 0ull : ~0ull);
    const unsigned long long wsa = (unsigned long long)p.ws;
    const u32x4 dsc = {(unsigned)wsa, (unsigned)(wsa >> 32) & 0xffffu, WS_BYTES, 0x00020000u};
    u32x2 tg; unsigned cnt;
    asm volatile(
        "s_cmp_eq_u64 %[bad], 0\n\ts_cbranch_scc1 2f\n\ts_mov_b32 %[cnt], 0x100000\n"
        "1:\n\ts_sleep 2\n\tbuffer_load_dwordx2 %[tg], %[vo], %[rs], %[so] offen offset:8 sc1\n\ts_waitcnt vmcnt(0)\n\t"
        "v_cmp_ne_u64 vcc, %[T], %[tg]\n\ts_sub_u32 %[cnt], %[cnt], 1\n\ts_cbranch_scc1 3f\n\ts_nop 4\n\ts_cbranch_vccnz 1b\n"
        "3:\n\tbuffer_load_dwordx4 %[pk], %[vo], %[rs], %[so] offen sc1\n\ts_waitcnt vmcnt(0)\n2:"
        : [pk] "+v"(pk), [tg] "=&v"(tg), [cnt] "=&s"(cnt)
        : [bad] "s"(bad), [vo] "v"(kvv), [rs] "s"(dsc), [so] "s"(sx), [T] "s"(T) : "vcc", "scc", "memory");
    *reinterpret_cast<u32x2*>(&s_x[NOSELF && (w >> 1) == bi ? 8 : w][2 * lane]) = u32x2{pk[0], pk[1]};
  }
  STV(1);
  lds_barrier();
  unsigned qp[4], hs[4], hr;
  {
    f32x4 q0 = *reinterpret_cast<const f32x4*>(&s_x[2 * r][8 * c]), q1 = *reinterpret_cast<const f32x4*>(&s_x[2 * r][8 * c + 4]);
    const float gu = xrow_sum(s_x[2 * r + 1][64 * (c >> 3) + 8 * w + c8]);
#pragma unroll
    for (int k = 0; k < 4; ++k) { q0[k] = xrow_sum(q0[k]); q1[k] = xrow_sum(q1[k]); }
    qp[0] = cvtpk(q0[0], q0[1]); qp[1] = cvtpk(q0[2], q0[3]); qp[2] = cvtpk(q1[0], q1[1]); qp[3] = cvtpk(q1[2], q1[3]);
    float hid = gu / (1.f + __expf(-gu)) * dpp<ROR8>(gu);
    KEEP(hid);
    hid = has_ffn ? hid : 0.f;
    const unsigned hp = cvtpk(hid, dpp<XOR1>(hid));
#pragma unroll
    for (int u = 0; u < 4; ++u) hs[u] = __builtin_amdgcn_readlane(hp, 2 * u);
    hr = row_pick<0>(hp);
    KEEP(hr);
  }
  PIN();
  constexpr float C2 = 0.08838834764831845f * 1.4426950408889634f;
  float sc[32], mw, f;
  float om[8] = {0.f, 0.f, 0.f, 0.f, 0.f, 0.f, 0.f, 0.f}, orr[8] = {0.f, 0.f, 0.f, 0.f, 0.f, 0.f, 0.f, 0.f};
  float acc[8] = {0.f, 0.f, 0.f, 0.f, 0.f, 0.f, 0.f, 0.f};
  unsigned pp[16];
  u32x2 wp[40];
  const unsigned rdoff = (lane < NCONS ? lane : 0) * 8;
  u32x2 rdy;
  sfor<JG + 1, 64>(LAM(J) {
    constexpr int j = decltype(J)::v;
#pragma unroll
    for (int t = kdone(j - 1); t < kdone(j); ++t) {
      float d = dot2(S[t][0], qp[0], 0.f);
      d = dot2(S[t][1], qp[1], d); d = dot2(S[t][2], qp[2], d); d = dot2(S[t][3], qp[3], d);
      d += dpp<XOR1>(d); d += dpp<XOR2>(d); d += dpp<XOR7>(d); d += dpp<ROR8>(d);
      sc[t] = d * C2;
      KEEP(sc[t]);
    }
#pragma unroll
    for (int u = wuse(j - 1); u < wuse(j); ++u) {
      if (u < 9) { pair_fma(Wt[2 * u], Wt[2 * u + 1], hs[u - 5], om); KEEP8(om); }
      else if (u < 10) { pair_fma(Wt[2 * u], Wt[2 * u + 1], hr, orr); KEEP8(orr); }
      else {
#pragma unroll
        for (int k = 0; k < 8; ++k) { wp[8 * ((u - 10) >> 1) + k][u & 1] = pcol(Wt[2 * u], Wt[2 * u + 1], k); KEEP(wp[8 * ((u - 10) >> 1) + k]); }
      }
    }
    if constexpr (j > 32 && (j & 1)) { pair_fma(S[j - 1], S[j], pp[(j - 33) >> 1], acc); KEEP8(acc); }
    PIN();
    if constexpr (j >= JG + 1 + DX) step(IC<j + DS>{});
    if constexpr (j == 48) { rdy = ld8<AUX_SC1>(rws0, rdoff, WS_READY + b * 256); PIN(); }
    if constexpr (j == 31) {
      mw = sc[0];
#pragma unroll
      for (int t = 1; t < 32; ++t) mw = fmaxf(mw, sc[t]);
      mw = xrow_max(mw);
      float lw = 0.f;
#pragma unroll
      for (int t = 0; t < 16; ++t) {
        const float e0 = __builtin_amdgcn_exp2f(sc[2 * t] - mw), e1 = __builtin_amdgcn_exp2f(sc[2 * t + 1] - mw);
        lw += e0 + e1; pp[t] = cvtpk(e0, e1); KEEP(pp[t]);
      }
      lw = xrow_sum(lw);
      s_ml[w] = mw; s_ml[8 + w] = lw;
      PIN();
    }
    if constexpr (j == JB2) {
      lds_barrier();
      STV(2);
      const f32x4 m0 = *reinterpret_cast<const f32x4*>(&s_ml[0]), m1 = *reinterpret_cast<const f32x4*>(&s_ml[4]);
      const f32x4 l0 = *reinterpret_cast<const f32x4*>(&s_ml[8]), l1 = *reinterpret_cast<const f32x4*>(&s_ml[12]);
      const float M = fmaxf(fmaxf(fmaxf(m0[0], m0[1]), fmaxf(m0[2], m0[3])), fmaxf(fmaxf(m1[0], m1[1]), fmaxf(m1[2], m1[3])));
      float L = 0.f;
#pragma unroll
      for (int k = 0; k < 4; ++k) L += l0[k] * __builtin_amdgcn_exp2f(m0[k] - M) + l1[k] * __builtin_amdgcn_exp2f(m1[k] - M);
      f = __builtin_amdgcn_exp2f(mw - M) / L;
      KEEP(f);
      PIN();
    }
    if constexpr (j == 50) {
#pragma unroll
      for (int k = 0; k < 8; ++k) { om[k] *= 1.f - g; orr[k] *= 1.f - g; }
      KEEP8(om); KEEP8(orr);
      PIN();
    }
  });
  *reinterpret_cast<f32x2*>(&s_q[w][8 * c + 2 * r]) = xrow_red8(acc) * f;
  STV(3);
  lds_barrier();
  STV(4);
  {
    const float o = xrow_sum(s_q[2 * r][16 * w + c] + s_q[2 * r + 1][16 * w + c]);
    const unsigned op = cvtpk(o, dpp<XOR1>(o));
    f32x4 dm[8], dr[8];
    const u32x2 cr = {row_pick<0>(op), row_pick<8>(op)};
#pragma unroll
    for (int k = 0; k < 8; ++k) { dr[k] = mma4(cr, wp[k], f32x4{0.f, 0.f, 0.f, 0.f}); dm[k] = f32x4{0.f, 0.f, 0.f, 0.f}; }
    sfor<0, 4>(LAM(Q) {
      constexpr int q = decltype(Q)::v;
      const u32x2 cm = {(unsigned)__builtin_amdgcn_mov_dpp((int)op, 0x150 + 4 * q, 0xf, 0xf, true), (unsigned)__builtin_amdgcn_mov_dpp((int)op, 0x152 + 4 * q, 0xf, 0xf, true)};
#pragma unroll
      for (int k = 0; k < 8; ++k) dm[k] = mma4(cm, wp[8 + 8 * q + k], dm[k]);
    });
    float am[8], ar[8];
#pragma unroll
    for (int k = 0; k < 8; ++k) { ar[k] = fmaf(g, dr[k][0], orr[k]); am[k] = fmaf(g, dm[k][0], om[k]); }
    *reinterpret_cast<f32x4*>(&s_red[w][8 * lane]) = f32x4{am[0], am[1], am[2], am[3]};
    *reinterpret_cast<f32x4*>(&s_red[w][8 * lane + 4]) = f32x4{am[4], am[5], am[6], am[7]};
    *reinterpret_cast<f32x2*>(&s_red[w][512 + 8 * c + 2 * r]) = xrow_red8(ar);
  }
  if (w == 0) {
#pragma nounroll
    for (unsigned spins = 0; spins < SPIN_CAP; ++spins) {
      if (__all((((unsigned long long)rdy[1] << 32) | rdy[0]) == T)) break;
      __builtin_amdgcn_s_sleep(4);
      rdy = ld8<AUX_SC1>(rws, rdoff, WS_READY + b * 256);
    }
  }
  lds_barrier();
  STV(5);
  if (tid < DM / 4) {
    f32x4 s = *reinterpret_cast<const f32x4*>(&s_red[0][4 * tid]);
#pragma unroll
    for (int k = 1; k < 8; ++k) s += *reinterpret_cast<const f32x4*>(&s_red[k][4 * tid]);
    u32x4 sv = __builtin_bit_cast(u32x4, s);
#pragma unroll
    for (int k = 0; k < 4; ++k) sv[k] = sv[k] == SENT ? 0x7FC00000u : sv[k];
    __builtin_amdgcn_raw_buffer_store_b128(sv, rws, (tid >> 3) * 4096 + (tid & 7) * 16, WS_MAIL + b * NCONS * 4096 + h * 128, MS);
    STNOP();
  }
  if (w == 3 && is_cons) {
    u32x4 m[4];
    bool ok[4] = {false, false, false, false};
#pragma unroll
    for (int i = 0; i < 4; ++i) m[i] = u32x4{0u, 0u, 0u, 0u};
#pragma nounroll
    for (unsigned spins = 0; spins < SPIN_CAP; ++spins) {
      u32x4 t[4];
#pragma unroll
      for (int i = 0; i < 4; ++i) t[i] = ld16<ML>(rws, ok[i] ? WS_BYTES : lane * 16 + i * 1024, WS_MAIL + (b * NCONS + cj) * 4096);
#pragma unroll
      for (int i = 0; i < 4; ++i) {
        const bool v = t[i][0] != SENT && t[i][1] != SENT && t[i][2] != SENT && t[i][3] != SENT;
        m[i] |= v ? t[i] : u32x4{0u, 0u, 0u, 0u};
        ok[i] = v;
      }
      if (__all(ok[0] && ok[1] && ok[2] && ok[3])) break;
      if (PSL) __builtin_amdgcn_s_sleep(PSL);
    }
    f32x4 s = (__builtin_bit_cast(f32x4, m[0]) + __builtin_bit_cast(f32x4, m[1])) + (__builtin_bit_cast(f32x4, m[2]) + __builtin_bit_cast(f32x4, m[3]));
#pragma unroll
    for (int k = 0; k < 4; ++k) { s[k] += dpp<ROR8>(s[k]); s[k] = xrow_sum(s[k]); }
    if (lane < 8) {
      const u32x2 ov = {cvtpk(s[0], s[1]), cvtpk(s[2], s[3])};
      *reinterpret_cast<u32x2*>(p.out + b * DM + cj * 32 + lane * 4) = ov;
    }
    ST(8, true);
    if (cj == 0) {
      const u32x2 e = ld8<AUX_SC1>(rws, 0, WS_READY + b * 256);
      if (lane == 0) __builtin_amdgcn_raw_buffer_store_b64(e, rws, 0, WS_EPOCH + b * 256, AUX_SC1);
    }
  }
}

extern "C" void kernel_launch(void* const* d_in, const int* in_sizes, int n_in, void* d_out, int out_size, void* d_ws,
                              size_t ws_size, hipStream_t stream) {
  Params p;
  p.x = (const uint16_t*)d_in[0]; p.k = (const uint16_t*)d_in[1]; p.v = (const uint16_t*)d_in[2];
  p.wq = (const uint16_t*)d_in[3]; p.wo = (const uint16_t*)d_in[4];
  p.w1 = (const uint16_t*)d_in[6]; p.w3 = (const uint16_t*)d_in[7]; p.w2 = (const uint16_t*)d_in[8];
  p.gw = (const uint16_t*)d_in[9];
  p.out = (uint16_t*)d_out; p.ws = (unsigned char*)d_ws;
  hipLaunchKernelGGL(gated_mix_decode_kernel, dim3(NB * NH), dim3(512), 0, stream, p);
}
```

```cpp
#include <hip/hip_runtime.h>
#include <hip/hip_bf16.h>
#include <stdint.h>

typedef __attribute__((ext_vector_type(2))) __bf16 bf16x2;
typedef __attribute__((ext_vector_type(4))) unsigned u32x4;
typedef __attribute__((ext_vector_type(2))) unsigned u32x2;
typedef __attribute__((ext_vector_type(4))) float f32x4;
typedef __attribute__((ext_vector_type(2))) float f32x2;
typedef __amdgpu_buffer_rsrc_t rsrc_t;

constexpr int NB = 8, DM = 640, NH = 32, SK = 4096, DH = 128, WIN = 1024, FF = 1728;
constexpr int NFS = FF / 64;
constexpr int NCONS = DM / 32;
constexpr int DS = 6, DX = 2;
constexpr int BS = 9;
constexpr int JG = 18, JB2 = 35;
constexpr int JW2 = 8, JWO = 20;
constexpr int D0 = 5;
constexpr int wcnt(int j) { return j < 0 ? 0 : j < JW2 ? 10 : j < JW2 + 10 ? j - JW2 + 11 : j < JWO ? 20 : (21 + (j - JWO) / 2 < 40 ? 21 + (j - JWO) / 2 : 40); }
constexpr int wuse(int j) { return j <= JG ? 5 : (5 + (j - JG) < wcnt(j) / 2 ? 5 + (j - JG) : wcnt(j) / 2); }
constexpr int kdone(int j) { return 3 * (j - JG) < j + 1 && 3 * (j - JG) < 32 ? 3 * (j - JG) : (j + 1 < 32 ? j + 1 : 32); }
constexpr int AUX_NT = 2, AUX_SC1 = 16;
constexpr unsigned SENT = 0xFFFFFFFFu;
constexpr unsigned SPIN_CAP = 1u << 20;
constexpr unsigned WS_EPOCH = 0;
constexpr unsigned WS_READY = 4096;
constexpr unsigned WS_MAIL = 8192;
constexpr unsigned WS_X = WS_MAIL + NB * NCONS * NH * 128;
constexpr unsigned WS_BYTES = WS_X + NB * NH * 8192;

__device__ __forceinline__ rsrc_t mkrsrc(const void* p, unsigned bytes) {
  return __builtin_amdgcn_make_buffer_rsrc(const_cast<void*>(p), 0, bytes, 0x00020000);
}
template <int AUX> __device__ __forceinline__ u32x4 ld16(rsrc_t r, unsigned voff, unsigned soff) {
  return __builtin_amdgcn_raw_buffer_load_b128(r, voff, soff, AUX);
}
template <int AUX> __device__ __forceinline__ u32x2 ld8(rsrc_t r, unsigned voff, unsigned soff) {
  return __builtin_amdgcn_raw_buffer_load_b64(r, voff, soff, AUX);
}
template <int AUX> __device__ __forceinline__ unsigned ld4(rsrc_t r, unsigned voff, unsigned soff) {
  return __builtin_amdgcn_raw_buffer_load_b32(r, voff, soff, AUX);
}
__device__ __forceinline__ float dot2(unsigned a, unsigned b, float acc) {
  return __builtin_amdgcn_fdot2_f32_bf16(__builtin_bit_cast(bf16x2, a), __builtin_bit_cast(bf16x2, b), acc, false);
}
__device__ __forceinline__ unsigned cvtpk(float lo, float hi) {
  bf16x2 v = {(__bf16)lo, (__bf16)hi};
  return __builtin_bit_cast(unsigned, v);
}
__device__ __forceinline__ unsigned plo(unsigned a, unsigned b) { return __builtin_amdgcn_perm(b, a, 0x05040100u); }
__device__ __forceinline__ unsigned phi(unsigned a, unsigned b) { return __builtin_amdgcn_perm(b, a, 0x07060302u); }
template <int CTRL> __device__ __forceinline__ float dpp(float x) {
  return __builtin_bit_cast(float, __builtin_amdgcn_mov_dpp(__builtin_bit_cast(int, x), CTRL, 0xf, 0xf, true));
}
constexpr int XOR1 = 0xB1, XOR2 = 0x4E, XOR7 = 0x141, ROR8 = 0x128;
__device__ __forceinline__ float xrow_sum(float x) {
  auto s = __builtin_amdgcn_permlane16_swap(__float_as_uint(x), __float_as_uint(x), false, false);
  x = __uint_as_float(s[0]) + __uint_as_float(s[1]);
  auto t = __builtin_amdgcn_permlane32_swap(__float_as_uint(x), __float_as_uint(x), false, false);
  return __uint_as_float(t[0]) + __uint_as_float(t[1]);
}
__device__ __forceinline__ float xrow_max(float x) {
  auto s = __builtin_amdgcn_permlane16_swap(__float_as_uint(x), __float_as_uint(x), false, false);
  x = fmaxf(__uint_as_float(s[0]), __uint_as_float(s[1]));
  auto t = __builtin_amdgcn_permlane32_swap(__float_as_uint(x), __float_as_uint(x), false, false);
  return fmaxf(__uint_as_float(t[0]), __uint_as_float(t[1]));
}
__device__ __forceinline__ float xrow_red4(float a, float b, float c, float d) {
  auto s = __builtin_amdgcn_permlane16_swap(__float_as_uint(a), __float_as_uint(b), false, false);
  auto t = __builtin_amdgcn_permlane16_swap(__float_as_uint(c), __float_as_uint(d), false, false);
  const float x = __uint_as_float(s[0]) + __uint_as_float(s[1]), y = __uint_as_float(t[0]) + __uint_as_float(t[1]);
  auto u = __builtin_amdgcn_permlane32_swap(__float_as_uint(x), __float_as_uint(y), false, false);
  return __uint_as_float(u[0]) + __uint_as_float(u[1]);
}
__device__ __forceinline__ f32x2 xrow_red8(const float* a) {
  return f32x2{xrow_red4(a[0], a[2], a[4], a[6]), xrow_red4(a[1], a[3], a[5], a[7])};
}
template <int L0> __device__ __forceinline__ unsigned row_pick(unsigned x) {
  int y = __builtin_amdgcn_mov_dpp((int)x, 0x150 + L0, 0xf, 0xf, true);
  y = __builtin_amdgcn_update_dpp(y, (int)x, 0x150 + L0 + 2, 0x2, 0xf, false);
  y = __builtin_amdgcn_update_dpp(y, (int)x, 0x150 + L0 + 4, 0x4, 0xf, false);
  y = __builtin_amdgcn_update_dpp(y, (int)x, 0x150 + L0 + 6, 0x8, 0xf, false);
  return (unsigned)y;
}
#define KEEP(x) asm volatile("" : "+v"(x))
#define KEEP8(a) asm volatile("" : "+v"(a[0]), "+v"(a[1]), "+v"(a[2]), "+v"(a[3]), "+v"(a[4]), "+v"(a[5]), "+v"(a[6]), "+v"(a[7]))
__device__ __forceinline__ void pair_fma(const u32x4& A, const u32x4& Bv, unsigned c, float* acc) {
#pragma unroll
  for (int d = 0; d < 4; ++d) {
    acc[2 * d] = dot2(plo(A[d], Bv[d]), c, acc[2 * d]);
    acc[2 * d + 1] = dot2(phi(A[d], Bv[d]), c, acc[2 * d + 1]);
  }
}
typedef __attribute__((ext_vector_type(4))) short s16x4;
__device__ __forceinline__ f32x4 mma4(u32x2 a, u32x2 bv, f32x4 d) {
  return __builtin_amdgcn_mfma_f32_4x4x4bf16_1k(__builtin_bit_cast(s16x4, a), __builtin_bit_cast(s16x4, bv), d, 0, 0, 0);
}
__device__ __forceinline__ unsigned pcol(const u32x4& A, const u32x4& Bv, int k) { return (k & 1) ? phi(A[k >> 1], Bv[k >> 1]) : plo(A[k >> 1], Bv[k >> 1]); }
__device__ __forceinline__ void lds_barrier() {
  asm volatile("s_waitcnt lgkmcnt(0)\n\ts_barrier" ::: "memory");
}
template <int I> struct IC { static constexpr int v = I; };
template <int A, int B, class F> __device__ __forceinline__ void sfor(F&& f) {
  if constexpr (A < B) { f(IC<A>{}); sfor<A + 1, B>(f); }
}
#define LAM(x) [&](auto x) __attribute__((always_inline))
#define PIN() __builtin_amdgcn_sched_barrier(0)
#define STNOP() do { PIN(); asm volatile("s_nop 3"); PIN(); } while (0)

struct Params {
  const uint16_t* x; const uint16_t* k; const uint16_t* v; const uint16_t* wq; const uint16_t* wo;
  const uint16_t* w1; const uint16_t* w3; const uint16_t* w2; const uint16_t* gw;
  uint16_t* out; unsigned char* ws;
};

__global__ void __launch_bounds__(512, 2) gated_mix_decode_kernel(Params p) {
  __shared__ __attribute__((aligned(16))) float s_q[8][DH];
  __shared__ __attribute__((aligned(16))) float s_x[9][128];
  __shared__ __attribute__((aligned(16))) float s_red[8][DM];
  __shared__ __attribute__((aligned(16))) float s_ml[16];
  __shared__ u32x2 s_ep[5];

  const int tid = threadIdx.x, lane = tid & 63, r = lane >> 4, c = lane & 15;
  const int w = __builtin_amdgcn_readfirstlane(tid >> 6);
  const int bid = blockIdx.x;
  const int cj = bid & 31, b = bid >> 5;
  const int h = cj;
  const int f0 = ((NFS * (bid & 7)) >> 3) + ((bid >> 3) & 3);
  const bool has_ffn = f0 < ((NFS * ((bid & 7) + 1)) >> 3), is_cons = cj < NCONS;

  const rsrc_t rws = mkrsrc(p.ws, WS_BYTES);
  const rsrc_t rx = mkrsrc(p.x, NB * DM * 2), rgw = mkrsrc(p.gw, DM * 2);
  const size_t kvoff = ((size_t)(b * NH + h) * SK + (SK - WIN)) * DH;
  const rsrc_t rk = mkrsrc(p.k + kvoff, WIN * DH * 2), rv = mkrsrc(p.v + kvoff, WIN * DH * 2);
  const rsrc_t rwo = mkrsrc(p.wo + (size_t)h * DH * DM, DH * DM * 2);
  const int fs = has_ffn ? f0 : 0;
  const rsrc_t rw2 = mkrsrc(p.w2 + (size_t)fs * 64 * DM, has_ffn ? 64 * DM * 2 : 0);
  const rsrc_t rws0 = mkrsrc(p.ws, w == 0 ? WS_BYTES : 0), rwsc = mkrsrc(p.ws, (w == 0 && is_cons) ? WS_BYTES : 0);

  const int bg = b & 4, bi = b & 3, wr = w & 3, wm = w >> 2;
  u32x2 ep = ld8<AUX_SC1>(rws0, lane < 4 ? (bg + lane) * 256 : WS_BYTES, WS_EPOCH);
  PIN();
  const int c8 = lane & 7, rb = 160 * bi + 40 * wr + 10 * r;
  u32x4 ga4, go4;
  unsigned ga1, go1;
  const u32x4 xa = ld16<0>(rx, rb * 2 + (c & 3) * (DM * 2), bg * (DM * 2));
  const unsigned xb = ld4<0>(rx, rb * 2 + 16 + (c & 3) * (DM * 2), bg * (DM * 2));
  PIN();
  u32x4 S[64], Wt[40], pk;
  const unsigned kvv = w * 1024 + lane * 16;
  const unsigned wst = wm ? FF * 2 : NH * DH * 2;
  const unsigned char* wptr = (wm ? (const unsigned char*)(c < 8 ? p.w1 : p.w3) + fs * 128 + c8 * 16 : (const unsigned char*)p.wq + h * 256 + c * 16) + (size_t)rb * wst;
  const unsigned vm = lane * 16, vr = 2 * r * (DM * 2) + 1024 + c * 16;
  const unsigned s2 = 8 * w * (DM * 2), so = 16 * w * (DM * 2), sx = WS_X + (b * NH + cj) * 8192;
  auto wload = [&](int i) __attribute__((always_inline)) -> u32x4 {
    if (i < 10) return __builtin_nontemporal_load((const __attribute__((address_space(1))) u32x4*)(wptr + i * wst));
    if (i < 18) return ld16<0>(rw2, vm + (i - 10) * (DM * 2), s2);
    if (i < 20) return ld16<0>(rw2, vr + (i - 18) * (DM * 2), s2);
    if (i < 24) return ld16<0>(rwo, vr + ((i & 1) + 8 * ((i - 20) >> 1)) * (DM * 2), so);
    return ld16<0>(rwo, vm + (i - 24) * (DM * 2), so);
  };
  auto step = LAM(J) {
    constexpr int j = decltype(J)::v;
    if constexpr (j < 64) {
      sfor<wcnt(j - 1), wcnt(j)>(LAM(I) { Wt[decltype(I)::v] = wload(decltype(I)::v); PIN(); });
      if constexpr (j == 0) { asm volatile("s_barrier"); PIN(); }
      S[j] = j < 32 ? ld16<AUX_NT>(rk, kvv, j * 8192) : ld16<AUX_NT>(rv, kvv, (j - 32) * 8192);
      PIN();
      if constexpr (j == JG) { pk = ld16<AUX_SC1 | AUX_NT>(mkrsrc(p.ws, (w >> 1) == bi ? 0 : WS_BYTES), kvv, sx); PIN(); }
      if constexpr (j % BS == BS - 1) { asm volatile("s_barrier"); PIN(); }
    }
  };
  sfor<0, D0>(step);
  {
    f32x4 a[8];
#pragma unroll
    for (int k = 0; k < 8; ++k) a[k] = mma4(u32x2{xa[0], xa[1]}, u32x2{pcol(Wt[0], Wt[1], k), pcol(Wt[2], Wt[3], k)}, f32x4{0.f, 0.f, 0.f, 0.f});
    KEEP8(a); PIN();
#pragma unroll
    for (int k = 0; k < 8; ++k) a[k] = mma4(u32x2{xa[2], xa[3]}, u32x2{pcol(Wt[4], Wt[5], k), pcol(Wt[6], Wt[7], k)}, a[k]);
    KEEP8(a); PIN();
#pragma unroll
    for (int k = 0; k < 8; ++k) a[k] = mma4(u32x2{xb, 0u}, u32x2{pcol(Wt[8], Wt[9], k), 0u}, a[k]);
    f32x4 z0, z1;
#pragma unroll
    for (int k = 0; k < 4; ++k) { z0[k] = xrow_red4(a[k][0], a[k][1], a[k][2], a[k][3]); z1[k] = xrow_red4(a[4 + k][0], a[4 + k][1], a[4 + k][2], a[4 + k][3]); }
    *reinterpret_cast<f32x4*>(&s_red[w][128 * r + 8 * c]) = z0;
    *reinterpret_cast<f32x4*>(&s_red[w][128 * r + 8 * c + 4]) = z1;
  }
  s_ep[w == 0 && lane < 4 ? lane : 4] = ep;
  lds_barrier();
  ep = s_ep[(lane & 1) ? wr : bi];
  KEEP(ep);
  const unsigned long long T = (((unsigned long long)(unsigned)__builtin_amdgcn_readlane(ep[1], 0) << 32) | (unsigned)__builtin_amdgcn_readlane(ep[0], 0)) + 1ull;
  {
    const unsigned long long Td = (((unsigned long long)(unsigned)__builtin_amdgcn_readlane(ep[1], 1) << 32) | (unsigned)__builtin_amdgcn_readlane(ep[0], 1)) + 1ull;
    f32x2 v = *reinterpret_cast<const f32x2*>(&s_red[4 * wm][128 * wr + 2 * lane]);
#pragma unroll
    for (int k = 1; k < 4; ++k) v += *reinterpret_cast<const f32x2*>(&s_red[4 * wm + k][128 * wr + 2 * lane]);
    const u32x4 o = {__float_as_uint(v[0]), (unsigned)Td, __float_as_uint(v[1]), (unsigned)Td + (unsigned)(Td >> 32)};
    __builtin_amdgcn_raw_buffer_store_b128(o, mkrsrc(p.ws, wr == bi ? 0 : WS_BYTES), vm, WS_X + ((bg + wr) * NH + cj) * 8192 + (2 * bi + wm) * 1024, AUX_SC1 | AUX_NT);
    STNOP();
    *reinterpret_cast<f32x2*>(&s_x[wr == bi ? 2 * bi + wm : 8][2 * lane]) = v;
    PIN();
  }
  {
    const u32x4 sv = {SENT, SENT, SENT, SENT};
#pragma unroll
    for (int i = 0; i < 4; ++i)
      __builtin_amdgcn_raw_buffer_store_b128(sv, rwsc, lane * 16 + i * 1024, WS_MAIL + (b * NCONS + cj) * 4096, AUX_SC1);
  }
  STNOP();
  ga4 = ld16<0>(rgw, lane * 20, 0); ga1 = ld4<0>(rgw, lane * 20 + 16, 0);
  go4 = ld16<0>(rx, lane * 20, b * (DM * 2)); go1 = ld4<0>(rx, lane * 20 + 16, b * (DM * 2));
  PIN();
  sfor<D0, JG + 1 + DS + DX>(step);
  float g;
  {
    float z = dot2(go1, ga1, 0.f);
#pragma unroll
    for (int k = 0; k < 4; ++k) z = dot2(go4[k], ga4[k], z);
    z += dpp<XOR1>(z); z += dpp<XOR2>(z); z += dpp<XOR7>(z); z += dpp<ROR8>(z);
    g = 1.f / (1.f + __expf(-xrow_sum(z)));
    KEEP(g);
    PIN();
  }
  {
    const unsigned ta = (unsigned)T, tb = (unsigned)T + (unsigned)(T >> 32);
    const unsigned long long bad = __builtin_amdgcn_ballot_w64(pk[1] != ta || pk[3] != tb) & ((w >> 1) == bi ? 0ull : ~0ull);
    const unsigned long long wsa = (unsigned long long)p.ws;
    const u32x4 dsc = {(unsigned)wsa, (unsigned)(wsa >> 32) & 0xffffu, WS_BYTES, 0x00020000u};
    unsigned t0, t1, cnt;
    asm volatile(
        "s_cmp_eq_u64 %[bad], 0\n\ts_cbranch_scc1 2f\n\ts_mov_b32 %[cnt], 0x100000\n"
        "1:\n\ts_sleep 2\n\tbuffer_load_dword %[t0], %[vo], %[rs], %[so] offen offset:4 sc1\n\t"
        "buffer_load_dword %[t1], %[vo], %[rs], %[so] offen offset:12 sc1\n\ts_waitcnt vmcnt(0)\n\t"
        "v_xor_b32 %[t0], %[ta], %[t0]\n\tv_xor_b32 %[t1], %[tb], %[t1]\n\tv_or_b32 %[t0], %[t0], %[t1]\n\t"
        "v_cmp_ne_u32 vcc, 0, %[t0]\n\ts_sub_u32 %[cnt], %[cnt], 1\n\ts_cbranch_scc1 3f\n\ts_nop 4\n\ts_cbranch_vccnz 1b\n"
        "3:\n\tbuffer_load_dwordx4 %[pk], %[vo], %[rs], %[so] offen sc1\n\ts_waitcnt vmcnt(0)\n2:"
        : [pk] "+v"(pk), [t0] "=&v"(t0), [t1] "=&v"(t1), [cnt] "=&s"(cnt)
        : [bad] "s"(bad), [vo] "v"(kvv), [rs] "s"(dsc), [so] "s"(sx), [ta] "s"(ta), [tb] "s"(tb) : "vcc", "scc", "memory");
    *reinterpret_cast<u32x2*>(&s_x[(w >> 1) == bi ? 8 : w][2 * lane]) = u32x2{pk[0], pk[2]};
  }
  lds_barrier();
  {
    const u32x2 tv = {(unsigned)T, (unsigned)(T >> 32)};
    __builtin_amdgcn_raw_buffer_store_b64(tv, rwsc, lane == 0 ? cj * 8 : WS_BYTES, WS_READY + b * 256, AUX_SC1);
  }
  unsigned qp[4], hs[4], hr;
  {
    f32x4 q0 = *reinterpret_cast<const f32x4*>(&s_x[2 * r][8 * c]), q1 = *reinterpret_cast<const f32x4*>(&s_x[2 * r][8 * c + 4]);
    const float gu = xrow_sum(s_x[2 * r + 1][64 * (c >> 3) + 8 * w + c8]);
#pragma unroll
    for (int k = 0; k < 4; ++k) { q0[k] = xrow_sum(q0[k]); q1[k] = xrow_sum(q1[k]); }
    qp[0] = cvtpk(q0[0], q0[1]); qp[1] = cvtpk(q0[2], q0[3]); qp[2] = cvtpk(q1[0], q1[1]); qp[3] = cvtpk(q1[2], q1[3]);
    float hid = gu / (1.f + __expf(-gu)) * dpp<ROR8>(gu);
    KEEP(hid);
    hid = has_ffn ? hid : 0.f;
    const unsigned hp = cvtpk(hid, dpp<XOR1>(hid));
#pragma unroll
    for (int u = 0; u < 4; ++u) hs[u] = __builtin_amdgcn_readlane(hp, 2 * u);
    hr = row_pick<0>(hp);
    KEEP(hr);
  }
  PIN();
  constexpr float C2 = 0.08838834764831845f * 1.4426950408889634f;
  float sc[32], mw, f;
  float om[8] = {0.f, 0.f, 0.f, 0.f, 0.f, 0.f, 0.f, 0.f}, orr[8] = {0.f, 0.f, 0.f, 0.f, 0.f, 0.f, 0.f, 0.f};
  float acc[8] = {0.f, 0.f, 0.f, 0.f, 0.f, 0.f, 0.f, 0.f};
  unsigned pp[16];
  u32x2 wp[40];
  const unsigned rdoff = (lane < NCONS ? lane : 0) * 8;
  u32x2 rdy;
  sfor<JG + 1, 64>(LAM(J) {
    constexpr int j = decltype(J)::v;
#pragma unroll
    for (int t = kdone(j - 1); t < kdone(j); ++t) {
      float d = dot2(S[t][0], qp[0], 0.f);
      d = dot2(S[t][1], qp[1], d); d = dot2(S[t][2], qp[2], d); d = dot2(S[t][3], qp[3], d);
      d += dpp<XOR1>(d); d += dpp<XOR2>(d); d += dpp<XOR7>(d); d += dpp<ROR8>(d);
      sc[t] = d * C2;
      KEEP(sc[t]);
    }
#pragma unroll
    for (int u = wuse(j - 1); u < wuse(j); ++u) {
      if (u < 9) { pair_fma(Wt[2 * u], Wt[2 * u + 1], hs[u - 5], om); KEEP8(om); }
      else if (u < 10) { pair_fma(Wt[2 * u], Wt[2 * u + 1], hr, orr); KEEP8(orr); }
      else {
#pragma unroll
        for (int k = 0; k < 8; ++k) { wp[8 * ((u - 10) >> 1) + k][u & 1] = pcol(Wt[2 * u], Wt[2 * u + 1], k); KEEP(wp[8 * ((u - 10) >> 1) + k]); }
      }
    }
    if constexpr (j > 32 && (j & 1)) { pair_fma(S[j - 1], S[j], pp[(j - 33) >> 1], acc); KEEP8(acc); }
    PIN();
    if constexpr (j >= JG + 1 + DX) step(IC<j + DS>{});
    if constexpr (j == 48) { rdy = ld8<AUX_SC1>(rws0, rdoff, WS_READY + b * 256); PIN(); }
    if constexpr (j == 31) {
      mw = sc[0];
#pragma unroll
      for (int t = 1; t < 32; ++t) mw = fmaxf(mw, sc[t]);
      mw = xrow_max(mw);
      float lw = 0.f;
#pragma unroll
      for (int t = 0; t < 16; ++t) {
        const float e0 = __builtin_amdgcn_exp2f(sc[2 * t] - mw), e1 = __builtin_amdgcn_exp2f(sc[2 * t + 1] - mw);
        lw += e0 + e1; pp[t] = cvtpk(e0, e1); KEEP(pp[t]);
      }
      lw = xrow_sum(lw);
      s_ml[w] = mw; s_ml[8 + w] = lw;
      PIN();
    }
    if constexpr (j == JB2) {
      lds_barrier();
      const f32x4 m0 = *reinterpret_cast<const f32x4*>(&s_ml[0]), m1 = *reinterpret_cast<const f32x4*>(&s_ml[4]);
      const f32x4 l0 = *reinterpret_cast<const f32x4*>(&s_ml[8]), l1 = *reinterpret_cast<const f32x4*>(&s_ml[12]);
      const float M = fmaxf(fmaxf(fmaxf(m0[0], m0[1]), fmaxf(m0[2], m0[3])), fmaxf(fmaxf(m1[0], m1[1]), fmaxf(m1[2], m1[3])));
      float L = 0.f;
#pragma unroll
      for (int k = 0; k < 4; ++k) L += l0[k] * __builtin_amdgcn_exp2f(m0[k] - M) + l1[k] * __builtin_amdgcn_exp2f(m1[k] - M);
      f = __builtin_amdgcn_exp2f(mw - M) / L;
      KEEP(f);
      PIN();
    }
    if constexpr (j == 50) {
#pragma unroll
      for (int k = 0; k < 8; ++k) { om[k] *= 1.f - g; orr[k] *= 1.f - g; }
      KEEP8(om); KEEP8(orr);
      PIN();
    }
  });
  *reinterpret_cast<f32x2*>(&s_q[w][8 * c + 2 * r]) = xrow_red8(acc) * f;
  lds_barrier();
  {
    const float o = xrow_sum(s_q[2 * r][16 * w + c] + s_q[2 * r + 1][16 * w + c]);
    const unsigned op = cvtpk(o, dpp<XOR1>(o));
    f32x4 dm[8], dr[8];
    const u32x2 cr = {row_pick<0>(op), row_pick<8>(op)};
#pragma unroll
    for (int k = 0; k < 8; ++k) { dr[k] = mma4(cr, wp[k], f32x4{0.f, 0.f, 0.f, 0.f}); dm[k] = f32x4{0.f, 0.f, 0.f, 0.f}; }
    sfor<0, 4>(LAM(Q) {
      constexpr int q = decltype(Q)::v;
      const u32x2 cm = {(unsigned)__builtin_amdgcn_mov_dpp((int)op, 0x150 + 4 * q, 0xf, 0xf, true), (unsigned)__builtin_amdgcn_mov_dpp((int)op, 0x152 + 4 * q, 0xf, 0xf, true)};
#pragma unroll
      for (int k = 0; k < 8; ++k) dm[k] = mma4(cm, wp[8 + 8 * q + k], dm[k]);
    });
    float am[8], ar[8];
#pragma unroll
    for (int k = 0; k < 8; ++k) { ar[k] = fmaf(g, dr[k][0], orr[k]); am[k] = fmaf(g, dm[k][0], om[k]); }
    *reinterpret_cast<f32x4*>(&s_red[w][8 * lane]) = f32x4{am[0], am[1], am[2], am[3]};
    *reinterpret_cast<f32x4*>(&s_red[w][8 * lane + 4]) = f32x4{am[4], am[5], am[6], am[7]};
    *reinterpret_cast<f32x2*>(&s_red[w][512 + 8 * c + 2 * r]) = xrow_red8(ar);
  }
  if (w == 0) {
#pragma nounroll
    for (unsigned spins = 0; spins < SPIN_CAP; ++spins) {
      if (__all((((unsigned long long)rdy[1] << 32) | rdy[0]) == T)) break;
      __builtin_amdgcn_s_sleep(4);
      rdy = ld8<AUX_SC1>(rws, rdoff, WS_READY + b * 256);
    }
  }
  lds_barrier();
  if (tid < DM / 4) {
    f32x4 s = *reinterpret_cast<const f32x4*>(&s_red[0][4 * tid]);
#pragma unroll
    for (int k = 1; k < 8; ++k) s += *reinterpret_cast<const f32x4*>(&s_red[k][4 * tid]);
    u32x4 sv = __builtin_bit_cast(u32x4, s);
#pragma unroll
    for (int k = 0; k < 4; ++k) sv[k] = sv[k] == SENT ? 0x7FC00000u : sv[k];
    __builtin_amdgcn_raw_buffer_store_b128(sv, rws, (tid >> 3) * 4096 + (tid & 7) * 16, WS_MAIL + b * NCONS * 4096 + h * 128, AUX_SC1);
    STNOP();
  }
  if (w == 3 && is_cons) {
    u32x4 m[4];
    bool ok[4] = {false, false, false, false};
#pragma unroll
    for (int i = 0; i < 4; ++i) m[i] = u32x4{0u, 0u, 0u, 0u};
#pragma nounroll
    for (unsigned spins = 0; spins < SPIN_CAP; ++spins) {
      u32x4 t[4];
#pragma unroll
      for (int i = 0; i < 4; ++i) t[i] = ld16<AUX_SC1>(rws, ok[i] ? WS_BYTES : lane * 16 + i * 1024, WS_MAIL + (b * NCONS + cj) * 4096);
#pragma unroll
      for (int i = 0; i < 4; ++i) {
        const bool v = t[i][0] != SENT && t[i][1] != SENT && t[i][2] != SENT && t[i][3] != SENT;
        m[i] |= v ? t[i] : u32x4{0u, 0u, 0u, 0u};
        ok[i] = v;
      }
      if (__all(ok[0] && ok[1] && ok[2] && ok[3])) break;
    }
    f32x4 s = (__builtin_bit_cast(f32x4, m[0]) + __builtin_bit_cast(f32x4, m[1])) + (__builtin_bit_cast(f32x4, m[2]) + __builtin_bit_cast(f32x4, m[3]));
#pragma unroll
    for (int k = 0; k < 4; ++k) { s[k] += dpp<ROR8>(s[k]); s[k] = xrow_sum(s[k]); }
    if (lane < 8) {
      const u32x2 ov = {cvtpk(s[0], s[1]), cvtpk(s[2], s[3])};
      __builtin_amdgcn_raw_buffer_store_b64(ov, mkrsrc(p.out, NB * DM * 2), lane * 8, (b * DM + cj * 32) * 2, 0);
    }
    if (cj == 0) {
      const u32x2 e = {(unsigned)T, (unsigned)(T >> 32)};
      if (lane == 0) __builtin_amdgcn_raw_buffer_store_b64(e, rws, 0, WS_EPOCH + b * 256, AUX_SC1);
    }
  }
}

extern "C" void kernel_launch(void* const* d_in, const int* in_sizes, int n_in, void* d_out, int out_size, void* d_ws,
                              size_t ws_size, hipStream_t stream) {
  Params p;
  p.x = (const uint16_t*)d_in[0]; p.k = (const uint16_t*)d_in[1]; p.v = (const uint16_t*)d_in[2];
  p.wq = (const uint16_t*)d_in[3]; p.wo = (const uint16_t*)d_in[4];
  p.w1 = (const uint16_t*)d_in[6]; p.w3 = (const uint16_t*)d_in[7]; p.w2 = (const uint16_t*)d_in[8];
  p.gw = (const uint16_t*)d_in[9];
  p.out = (uint16_t*)d_out; p.ws = (unsigned char*)d_ws;
  hipLaunchKernelGGL(gated_mix_decode_kernel, dim3(NB * NH), dim3(512), 0, stream, p);
}
```
